```python
import torch

def _softmax(x, axis=-1):
    return torch.softmax(x, dim=axis)
def _gelu(x):
    return torch.nn.functional.gelu(x, approximate="tanh")
def _silu(x):
    return torch.nn.functional.silu(x)
def _sigmoid(x):
    return torch.sigmoid(x)
def _layernorm(x, axis=-1, eps=1e-5):
    m = x.mean(dim=axis, keepdim=True)
    v = x.var(dim=axis, keepdim=True, unbiased=False)
    return (x - m) / torch.sqrt(v + eps)
def _scatter_add(tgt, idx, src, axis=0):
    return tgt.clone().index_add_(axis, idx, src)
def _segment_sum(src, seg, n):
    out = torch.zeros((n,) + src.shape[1:], dtype=src.dtype)
    return out.index_add_(0, seg, src)
def _pad(x, pw):
    flat = tuple(v for pair in reversed(pw) for v in pair)
    return torch.nn.functional.pad(x, flat)

kernel_name = 'recurrent_scan__ssm_conv_s5__mixer_token_mlp__prompt'

def setup_inputs():

    _w = (lambda a: (lambda t: t.to(torch.bfloat16) if t.is_floating_point() else t)(torch.as_tensor(a)))
    def _A_setup():
        B, S, D = 1, 2048, 512
        P = D
        Lambda_re = -0.5 * (lambda _x: torch.exp(torch.as_tensor(_x)))((lambda *s: torch.randn(*s, dtype=torch.bfloat16) * 0.4)(P))
        Lambda_im = (lambda *s: torch.randn(*s, dtype=torch.bfloat16) * 0.4)(P) * 1.0
        B_re = (lambda *s: torch.randn(*s, dtype=torch.bfloat16) * 0.4)(P, D) * (D ** -0.5)
        B_im = (lambda *s: torch.randn(*s, dtype=torch.bfloat16) * 0.4)(P, D) * (D ** -0.5)
        C_re = (lambda *s: torch.randn(*s, dtype=torch.bfloat16) * 0.4)(D, P) * (P ** -0.5)
        C_im = (lambda *s: torch.randn(*s, dtype=torch.bfloat16) * 0.4)(D, P) * (P ** -0.5)
        log_dt = (lambda *s: torch.randn(*s, dtype=torch.bfloat16) * 0.4)(P) * 0.3 - 2.0
        Dskip = (lambda *s: torch.randn(*s, dtype=torch.bfloat16) * 0.2)(D)
        return {
            "x": _w((lambda *s: torch.randn(*s, dtype=torch.bfloat16) * 0.05)(B, S, D)),
            "Lambda_re": _w(Lambda_re),
            "Lambda_im": _w(Lambda_im),
            "B_re": _w(B_re),
            "B_im": _w(B_im),
            "C_re": _w(C_re),
            "C_im": _w(C_im),
            "log_dt": _w(log_dt),
            "Dskip": _w(Dskip),
        }
    _A_in = _A_setup()

    def _B_setup():
        B, S, D, F = 1, 2048, 512, 1792
        return {
            "x": _w((lambda *s: torch.randn(*s, dtype=torch.bfloat16) * 0.05)(B, S, D)),
            "w1": _w((lambda *s: torch.randn(*s, dtype=torch.bfloat16) * 0.4)(S, F) * (S ** -0.5)),
            "b1": _w((lambda *s: torch.randn(*s, dtype=torch.bfloat16) * 3.2)(F) * 0.02),
            "w2": _w((lambda *s: torch.randn(*s, dtype=torch.bfloat16) * 0.4)(F, S) * (F ** -0.5)),
            "b2": _w((lambda *s: torch.randn(*s, dtype=torch.bfloat16) * 0.8)(S) * 0.02),
        }
    _B_in = _B_setup()

    return {
        "A_x": _A_in["x"],
        "A_Lambda_re": _A_in["Lambda_re"],
        "A_Lambda_im": _A_in["Lambda_im"],
        "A_B_re": _A_in["B_re"],
        "A_B_im": _A_in["B_im"],
        "A_C_re": _A_in["C_re"],
        "A_C_im": _A_in["C_im"],
        "A_log_dt": _A_in["log_dt"],
        "A_Dskip": _A_in["Dskip"],
        "B_x": _B_in["x"],
        "B_w1": _B_in["w1"],
        "B_b1": _B_in["b1"],
        "B_w2": _B_in["w2"],
        "B_b2": _B_in["b2"],
    }

def reference(A_x, A_Lambda_re, A_Lambda_im, A_B_re, A_B_im, A_C_re, A_C_im, A_log_dt, A_Dskip, B_x, B_w1, B_b1, B_w2, B_b2):
    def _A_ref(x, Lambda_re, Lambda_im, B_re, B_im, C_re, C_im, log_dt, Dskip):
        Bsz, L, H = x.shape
        Lam = Lambda_re + 1j * Lambda_im
        Bmat = B_re + 1j * B_im
        Cmat = C_re + 1j * C_im
        dt = torch.exp(log_dt)
        Lam_bar = torch.exp(dt * Lam)
        B_bar = ((Lam_bar - 1.0) / Lam)[:, None] * Bmat
        Bu = torch.einsum("bld,pd->blp", x + 0j, B_bar)
        h = torch.zeros_like(Bu[:, 0, :])
        hs = []
        for t in range(L):
            h = Lam_bar[None, :] * h + Bu[:, t, :]
            hs.append(h)
        hseq = torch.stack(hs, 1)
        y = 2.0 * torch.einsum("blp,dp->bld", hseq, Cmat).real + x * Dskip[None, None, :]
        return y

    def _B_ref(x, w1, b1, w2, b2):
        B, S, D = x.shape
        h = _layernorm(x, axis=-1)
        u = torch.einsum("bsd,sf->bdf", h, w1) + b1
        u = _gelu(u)
        y = torch.einsum("bdf,fs->bds", u, w2) + b2
        return (lambda x, *d: x.permute(*d))(y, 0, 2, 1)

    x = A_x
    xn = _layernorm(x, axis=-1)
    s = _A_ref(xn, A_Lambda_re, A_Lambda_im, A_B_re, A_B_im, A_C_re, A_C_im, A_log_dt, A_Dskip)
    g = _silu(_B_ref(xn, B_w1, B_b1, B_w2, B_b2))
    return x + s * g
```

```cpp
#include <hip/hip_runtime.h>
#include <stdint.h>

typedef unsigned short bf16_t;
typedef __attribute__((ext_vector_type(8))) short bf16x8;
typedef __attribute__((ext_vector_type(4))) short s16x4;
typedef __attribute__((ext_vector_type(16))) float f32x16;
typedef unsigned long long u64;

#define DI __device__ __forceinline__
#define GAS __attribute__((address_space(1)))
#define LAS __attribute__((address_space(3)))

constexpr int L_ = 2048;
constexpr int D_ = 512;
constexpr int P_ = 512;
constexpr int F_ = 1792;

DI float bf2f(bf16_t u) { return __uint_as_float(((unsigned)u) << 16); }
DI bf16_t f2bf(float f) { __bf16 b = (__bf16)f; return __builtin_bit_cast(bf16_t, b); }
DI float rbf(float f) { return bf2f(f2bf(f)); }

DI void glds16(const void* g, void* l) {
  __builtin_amdgcn_global_load_lds((const unsigned*)g, (unsigned*)l, 16, 0, 0);
}
#define MFMA32(a, b, c) __builtin_amdgcn_mfma_f32_32x32x16_bf16((a), (b), (c), 0, 0, 0)
#define BAR() __builtin_amdgcn_s_barrier()

DI void wait_vm(int n) {
  if (n >= 12)      asm volatile("s_waitcnt vmcnt(12)" ::: "memory");
  else if (n >= 10) asm volatile("s_waitcnt vmcnt(10)" ::: "memory");
  else if (n >= 8)  asm volatile("s_waitcnt vmcnt(8)" ::: "memory");
  else if (n >= 4)  asm volatile("s_waitcnt vmcnt(4)" ::: "memory");
  else              asm volatile("s_waitcnt vmcnt(0)" ::: "memory");
}

DI int crow(int reg, int h) { return (reg & 3) + 8 * (reg >> 2) + 4 * h; }

template <int R>
DI void stage_kc(const bf16_t* __restrict__ g, long ld, char* lds, int wave, int lane) {
  constexpr int PPW = R / 32;
#pragma unroll
  for (int i = 0; i < PPW; ++i) {
    const int piece = wave * PPW + i;
    const int row = piece * 8 + (lane >> 3);
    const int c = (lane & 7) ^ ((row >> 1) & 7);
    glds16(g + (long)row * ld + c * 8, lds + piece * 1024 + lane * 16);
  }
}
DI bf16x8 read_kc(const char* lds, int row, int ks, int h) {
  return *reinterpret_cast<const bf16x8*>(lds + row * 128 + (((2 * ks + h) ^ ((row >> 1) & 7)) << 4));
}
DI void stage_ks64(const bf16_t* __restrict__ g, long ld, char* lds, int wave, int lane) {
#pragma unroll
  for (int i = 0; i < 2; ++i) {
    const int piece = wave * 2 + i;
    const int k = piece * 8 + ((lane >> 2) & 7);
    const int col = (lane >> 5) * 32 + (lane & 3) * 8;
    glds16(g + (long)k * ld + col, lds + piece * 1024 + lane * 16);
  }
}
DI bf16x8 read_ks64(const char* lds, int cb, int ks, int lane) {
  const int h = lane >> 5, blk = (lane >> 4) & 1, q = (lane & 15) >> 2, p = lane & 3;
  const char* a = lds + (2 * ks + h) * 1024 + cb * 512 + q * 64 + blk * 32 + p * 8;
  s16x4 lo = __builtin_amdgcn_ds_read_tr16_b64_v4i16((LAS s16x4*)(a));
  s16x4 hi = __builtin_amdgcn_ds_read_tr16_b64_v4i16((LAS s16x4*)(a + 256));
  return __builtin_shufflevector(lo, hi, 0, 1, 2, 3, 4, 5, 6, 7);
}

DI float wave_sum(float v) {
#pragma unroll
  for (int o = 32; o > 0; o >>= 1) v += __shfl_xor(v, o);
  return v;
}
DI void ln_row(const float (&v)[8], float (&o)[8]) {
  float s = 0.f;
#pragma unroll
  for (int i = 0; i < 8; ++i) s += v[i];
  const float mean = wave_sum(s) * (1.0f / 512.0f);
  float q = 0.f;
#pragma unroll
  for (int i = 0; i < 8; ++i) { const float d = v[i] - mean; q += d * d; }
  const float var = wave_sum(q) * (1.0f / 512.0f);
  const float mb = rbf(mean), vb = rbf(var);
  const float den = rbf(sqrtf(rbf(vb + 1e-5f)));
#pragma unroll
  for (int i = 0; i < 8; ++i) o[i] = rbf(rbf(v[i] - mb) / den);
}

__global__ __launch_bounds__(256) void k1_ln(const bf16_t* __restrict__ x, bf16_t* __restrict__ xn, bf16_t* __restrict__ hn,
                                             const bf16_t* __restrict__ Lre, const bf16_t* __restrict__ Lim,
                                             const bf16_t* __restrict__ logdt, float4* __restrict__ cst,
                                             uint4* __restrict__ aggz) {
  const int tid = threadIdx.x, lane = tid & 63, w = tid >> 6, bid = blockIdx.x;
  if (tid < 16) aggz[bid * 16 + tid] = make_uint4(0u, 0u, 0u, 0u);
  if (bid < 2) {
    const int p = bid * 256 + tid;
    const float lre = bf2f(Lre[p]), lim = bf2f(Lim[p]);
    const float dt = rbf(expf(bf2f(logdt[p])));
    const float zr = dt * lre, zi = dt * lim;
    const float e = expf(zr);
    float sn, cs;
    sincosf(zi, &sn, &cs);
    const float br = e * cs, bi = e * sn;
    const float nr = br - 1.0f, ni = bi;
    const float den = lre * lre + lim * lim;
    cst[p] = make_float4(br, bi, (nr * lre + ni * lim) / den, (ni * lre - nr * lim) / den);
  }
  const int row = bid * 4 + w;
  const uint4 raw = reinterpret_cast<const uint4*>(x + (long)row * D_)[lane];
  const unsigned rw[4] = {raw.x, raw.y, raw.z, raw.w};
  float v[8], a[8], b[8];
#pragma unroll
  for (int i = 0; i < 4; ++i) { v[2 * i] = __uint_as_float(rw[i] << 16); v[2 * i + 1] = __uint_as_float(rw[i] & 0xffff0000u); }
  ln_row(v, a);
  ln_row(a, b);
  unsigned pa[4], pb[4];
#pragma unroll
  for (int i = 0; i < 4; ++i) {
    pa[i] = (unsigned)f2bf(a[2 * i]) | ((unsigned)f2bf(a[2 * i + 1]) << 16);
    pb[i] = (unsigned)f2bf(b[2 * i]) | ((unsigned)f2bf(b[2 * i + 1]) << 16);
  }
  reinterpret_cast<uint4*>(xn + (long)row * D_)[lane] = make_uint4(pa[0], pa[1], pa[2], pa[3]);
  reinterpret_cast<uint4*>(hn + (long)row * D_)[lane] = make_uint4(pb[0], pb[1], pb[2], pb[3]);
}

constexpr int NB1 = 224;
constexpr int NA1 = 128;
constexpr int K2_LDS = 65536 + 2048;

struct cplx { float r, i; };
DI cplx cmul(cplx a, cplx b) { return {a.r * b.r - a.i * b.i, a.r * b.i + a.i * b.r}; }
DI cplx cfma(cplx a, cplx b, cplx c) { return {c.r + a.r * b.r - a.i * b.i, c.i + a.r * b.i + a.i * b.r}; }

DI void b1_tile(int bid, const bf16_t* __restrict__ hn, const bf16_t* __restrict__ w1, const bf16_t* __restrict__ b1,
                bf16_t* __restrict__ u, char* smem) {
  const int tid = threadIdx.x, lane = tid & 63, wave = __builtin_amdgcn_readfirstlane(tid >> 6);
  const int wm = wave >> 1, wn = wave & 1, h = lane >> 5;
  const int lin = (bid & 7) * 28 + (bid >> 3);
  const int ft = lin >> 3, dt = lin & 7;
  const bf16_t* Ag = hn + dt * 64;
  const bf16_t* Bg = w1 + ft * 64;
  constexpr int S = 4, NT = L_ / 64, SB = 16384;
  f32x16 acc;
#pragma unroll
  for (int i = 0; i < 16; ++i) acc[i] = 0.f;
#pragma unroll
  for (int s = 0; s < S - 1; ++s) {
    stage_ks64(Ag + (long)s * 64 * D_, D_, smem + s * SB, wave, lane);
    stage_ks64(Bg + (long)s * 64 * F_, F_, smem + s * SB + 8192, wave, lane);
  }
  for (int t = 0; t < NT; ++t) {
    wait_vm(4 * min(S - 2, NT - 1 - t));
    BAR();
    if (t + S - 1 < NT) {
      char* nb = smem + ((t + S - 1) & (S - 1)) * SB;
      stage_ks64(Ag + (long)(t + S - 1) * 64 * D_, D_, nb, wave, lane);
      stage_ks64(Bg + (long)(t + S - 1) * 64 * F_, F_, nb + 8192, wave, lane);
    }
    const char* cb = smem + (t & (S - 1)) * SB;
#pragma unroll
    for (int ks = 0; ks < 4; ++ks) {
      const bf16x8 a = read_ks64(cb, wm, ks, lane);
      const bf16x8 b = read_ks64(cb + 8192, wn, ks, lane);
      acc = MFMA32(a, b, acc);
    }
  }
  const int f = ft * 64 + wn * 32 + (lane & 31);
  const float bias = bf2f(b1[f]);
#pragma unroll
  for (int r = 0; r < 16; ++r) {
    const int d = dt * 64 + wm * 32 + crow(r, h);
    float v = rbf(acc[r]);
    v = rbf(v + bias);
    const float inner = 0.7978845608028654f * (v + 0.044715f * v * v * v);
    const float g = 0.5f * v * (1.0f + tanhf(inner));
    u[(long)d * F_ + f] = f2bf(g);
  }
}

DI void a1_tile(int idx, const bf16_t* __restrict__ xn, const bf16_t* __restrict__ Bre, const bf16_t* __restrict__ Bim,
                const float4* __restrict__ cst, u64* agg, bf16_t* __restrict__ hbuf, char* smem) {
  const int tid = threadIdx.x, lane = tid & 63, wave = __builtin_amdgcn_readfirstlane(tid >> 6);
  const int wm = wave >> 1, wn = wave & 1, h = lane >> 5;
  const int tb = idx >> 3, cg = idx & 7;
  const bf16_t* Ag = xn + (long)tb * 128 * D_;
  constexpr int NT = D_ / 64, SB = 32768;
  f32x16 acc[2][2];
#pragma unroll
  for (int a = 0; a < 2; ++a)
#pragma unroll
    for (int b = 0; b < 2; ++b)
#pragma unroll
      for (int i = 0; i < 16; ++i) acc[a][b][i] = 0.f;

  auto issue = [&](int t) {
    char* nb = smem + (t & 1) * SB;
    stage_kc<128>(Ag + t * 64, D_, nb, wave, lane);
    stage_kc<64>(Bre + (long)cg * 64 * D_ + t * 64, D_, nb + 16384, wave, lane);
    stage_kc<64>(Bim + (long)cg * 64 * D_ + t * 64, D_, nb + 16384 + 8192, wave, lane);
  };
  issue(0);
  for (int t = 0; t < NT; ++t) {
    asm volatile("s_waitcnt vmcnt(0)" ::: "memory");
    BAR();
    if (t + 1 < NT) issue(t + 1);
    const char* cb = smem + (t & 1) * SB;
#pragma unroll
    for (int ks = 0; ks < 4; ++ks) {
      bf16x8 a[2], b[2];
#pragma unroll
      for (int m = 0; m < 2; ++m) a[m] = read_kc(cb, wm * 64 + m * 32 + (lane & 31), ks, h);
#pragma unroll
      for (int n = 0; n < 2; ++n) b[n] = read_kc(cb + 16384 + wn * 8192, n * 32 + (lane & 31), ks, h);
#pragma unroll
      for (int m = 0; m < 2; ++m)
#pragma unroll
        for (int n = 0; n < 2; ++n) acc[m][n] = MFMA32(a[m], b[n], acc[m][n]);
    }
  }
  __syncthreads();
  float* X = reinterpret_cast<float*>(smem);
  float2* E = reinterpret_cast<float2*>(smem + 65536);
#pragma unroll
  for (int m = 0; m < 2; ++m)
#pragma unroll
    for (int n = 0; n < 2; ++n)
#pragma unroll
      for (int r = 0; r < 16; ++r) {
        const int row = wm * 64 + m * 32 + crow(r, h);
        X[(wn * 128 + row) * 64 + n * 32 + (lane & 31)] = acc[m][n][r];
      }
  __syncthreads();
  const int ch = lane, seg = wave;
  const float4 c4 = cst[cg * 64 + ch];
  const cplx lam = {c4.x, c4.y}, coef = {c4.z, c4.w};
  cplx g = {0.f, 0.f};
  float* Xr = X + seg * 32 * 64 + ch;
  float* Xi = Xr + 128 * 64;
#pragma unroll 8
  for (int j = 0; j < 32; ++j) {
    const cplx xv = {Xr[j * 64], Xi[j * 64]};
    g = cfma(lam, g, xv);
    Xr[j * 64] = g.r;
    Xi[j * 64] = g.i;
  }
  E[seg * 64 + ch] = make_float2(g.r, g.i);
  __syncthreads();
  cplx L32 = lam;
#pragma unroll
  for (int i = 0; i < 5; ++i) L32 = cmul(L32, L32);
  cplx c = {0.f, 0.f}, cin = {0.f, 0.f};
#pragma unroll
  for (int s = 0; s < 4; ++s) {
    if (s == seg) cin = c;
    const float2 e = E[s * 64 + ch];
    c = cfma(L32, c, cplx{e.x, e.y});
  }
  if (seg == 0) {
    GAS u64* ag = (GAS u64*)(agg + ((long)tb * P_ + cg * 64 + ch) * 2);
    __hip_atomic_store(ag, (1ull << 32) | (u64)__float_as_uint(c.r), __ATOMIC_RELAXED, __HIP_MEMORY_SCOPE_AGENT);
    __hip_atomic_store(ag + 1, (1ull << 32) | (u64)__float_as_uint(c.i), __ATOMIC_RELAXED, __HIP_MEMORY_SCOPE_AGENT);
  }
  cplx L128 = cmul(L32, L32);
  L128 = cmul(L128, L128);
  cplx cb = {0.f, 0.f};
  for (unsigned spins = 0;; ++spins) {
    bool ok = true;
    cb = cplx{0.f, 0.f};
#pragma unroll
    for (int b = 0; b < 15; ++b) {
      if (b < tb) {
        GAS u64* ag = (GAS u64*)(agg + ((long)b * P_ + cg * 64 + ch) * 2);
        const u64 vr = __hip_atomic_load(ag, __ATOMIC_RELAXED, __HIP_MEMORY_SCOPE_AGENT);
        const u64 vi = __hip_atomic_load(ag + 1, __ATOMIC_RELAXED, __HIP_MEMORY_SCOPE_AGENT);
        ok = ok && ((vr >> 32) == 1ull) && ((vi >> 32) == 1ull);
        cb = cfma(L128, cb, cplx{__uint_as_float((unsigned)vr), __uint_as_float((unsigned)vi)});
      }
    }
    if (__all(ok)) break;
    if (spins > 200000u) break;
    __builtin_amdgcn_s_sleep(1);
  }
  cplx Pw = {1.f, 0.f};
  for (int s = 0; s < seg; ++s) Pw = cmul(Pw, L32);
  const cplx cs = cfma(Pw, cb, cin);
  cplx p = lam;
  bf16_t* hrow = hbuf + ((long)tb * 128 + seg * 32) * 2048 + cg * 64 + ch;
#pragma unroll 8
  for (int j = 0; j < 32; ++j) {
    cplx hv = {Xr[j * 64], Xi[j * 64]};
    hv = cfma(p, cs, hv);
    p = cmul(p, lam);
    hv = cmul(coef, hv);
    const float re = hv.r, nim = -hv.i;
    const bf16_t rh = f2bf(re), ih = f2bf(nim);
    bf16_t* o = hrow + (long)j * 2048;
    o[0] = rh;
    o[512] = ih;
    o[1024] = f2bf(re - bf2f(rh));
    o[1536] = f2bf(nim - bf2f(ih));
  }
}

__global__ __launch_bounds__(256, 2) void k2_gemm_scan(const bf16_t* __restrict__ xn, const bf16_t* __restrict__ hn,
                                                       const bf16_t* __restrict__ Bre, const bf16_t* __restrict__ Bim,
                                                       const float4* __restrict__ cst, u64* agg, bf16_t* __restrict__ hbuf,
                                                       const bf16_t* __restrict__ w1, const bf16_t* __restrict__ b1,
                                                       bf16_t* __restrict__ u) {
  __shared__ __attribute__((aligned(16))) char smem[K2_LDS];
  if (blockIdx.x < NB1) b1_tile(blockIdx.x, hn, w1, b1, u, smem);
  else a1_tile(blockIdx.x - NB1, xn, Bre, Bim, cst, agg, hbuf, smem);
}

constexpr int K3_SB = 24576, K3_S = 4;
__global__ __launch_bounds__(256) void k3_out(const bf16_t* __restrict__ hbuf, const bf16_t* __restrict__ Cre,
                                              const bf16_t* __restrict__ Cim, const bf16_t* __restrict__ u,
                                              const bf16_t* __restrict__ w2, const bf16_t* __restrict__ b2,
                                              const bf16_t* __restrict__ x, const bf16_t* __restrict__ xn,
                                              const bf16_t* __restrict__ Dsk, float* __restrict__ out) {
  __shared__ __attribute__((aligned(16))) char smem[K3_SB * K3_S];
  const int tid = threadIdx.x, lane = tid & 63, wave = __builtin_amdgcn_readfirstlane(tid >> 6);
  const int wm = wave >> 1, wn = wave & 1, h = lane >> 5;
  const int bid = blockIdx.x;
  const int st = (bid & 7) * 4 + (bid >> 6), dt = (bid >> 3) & 7;
  constexpr int NA = 16, NB = 28, NG = NA + NB;
  const bf16_t* hA = hbuf + (long)st * 64 * 2048;
  auto issue = [&](int g) {
    char* nb = smem + (g & (K3_S - 1)) * K3_SB;
    if (g < NA) {
      stage_kc<64>(hA + g * 64, 2048, nb, wave, lane);
      stage_kc<64>(hA + 1024 + g * 64, 2048, nb + 8192, wave, lane);
      stage_kc<64>((g < 8 ? Cre : Cim) + (long)dt * 64 * P_ + (g & 7) * 64, P_, nb + 16384, wave, lane);
    } else {
      const int kt = g - NA;
      stage_ks64(w2 + (long)kt * 64 * L_ + st * 64, L_, nb, wave, lane);
      stage_kc<64>(u + (long)dt * 64 * F_ + kt * 64, F_, nb + 8192, wave, lane);
    }
  };
  auto gcount = [&](int g) { return g >= NG ? 0 : (g < NA ? 6 : 4); };
  f32x16 acc1, acc2;
#pragma unroll
  for (int i = 0; i < 16; ++i) { acc1[i] = 0.f; acc2[i] = 0.f; }
#pragma unroll
  for (int s = 0; s < K3_S - 1; ++s) issue(s);
  for (int g = 0; g < NA; ++g) {
    wait_vm(gcount(g + 1) + gcount(g + 2));
    BAR();
    issue(g + K3_S - 1);
    const char* cb = smem + (g & (K3_S - 1)) * K3_SB;
#pragma unroll
    for (int ks = 0; ks < 4; ++ks) {
      const bf16x8 a0 = read_kc(cb, wm * 32 + (lane & 31), ks, h);
      const bf16x8 a1 = read_kc(cb + 8192, wm * 32 + (lane & 31), ks, h);
      const bf16x8 b = read_kc(cb + 16384, wn * 32 + (lane & 31), ks, h);
      acc1 = MFMA32(a0, b, acc1);
      acc1 = MFMA32(a1, b, acc1);
    }
  }
  for (int g = NA; g < NG; ++g) {
    wait_vm(gcount(g + 1) + gcount(g + 2));
    BAR();
    if (g + K3_S - 1 < NG) issue(g + K3_S - 1);
    const char* cb = smem + (g & (K3_S - 1)) * K3_SB;
#pragma unroll
    for (int ks = 0; ks < 4; ++ks) {
      const bf16x8 a = read_ks64(cb, wm, ks, lane);
      const bf16x8 b = read_kc(cb + 8192, wn * 32 + (lane & 31), ks, h);
      acc2 = MFMA32(a, b, acc2);
    }
  }
  const int d = dt * 64 + wn * 32 + (lane & 31);
  const float dsk = bf2f(Dsk[d]);
#pragma unroll
  for (int r = 0; r < 16; ++r) {
    const int s = st * 64 + wm * 32 + crow(r, h);
    const long o = (long)s * D_ + d;
    const float yA = 2.0f * acc1[r] + rbf(bf2f(xn[o]) * dsk);
    float yB = rbf(acc2[r]);
    yB = rbf(yB + bf2f(b2[s]));
    const float gg = rbf(yB / (1.0f + expf(-yB)));
    out[o] = bf2f(x[o]) + yA * gg;
  }
}

extern "C" void kernel_launch(void* const* d_in, const int* in_sizes, int n_in, void* d_out, int out_size, void* d_ws,
                              size_t ws_size, hipStream_t stream) {
  const bf16_t* x = (const bf16_t*)d_in[0];
  const bf16_t* Lre = (const bf16_t*)d_in[1];
  const bf16_t* Lim = (const bf16_t*)d_in[2];
  const bf16_t* Bre = (const bf16_t*)d_in[3];
  const bf16_t* Bim = (const bf16_t*)d_in[4];
  const bf16_t* Cre = (const bf16_t*)d_in[5];
  const bf16_t* Cim = (const bf16_t*)d_in[6];
  const bf16_t* logdt = (const bf16_t*)d_in[7];
  const bf16_t* Dsk = (const bf16_t*)d_in[8];
  const bf16_t* w1 = (const bf16_t*)d_in[10];
  const bf16_t* b1 = (const bf16_t*)d_in[11];
  const bf16_t* w2 = (const bf16_t*)d_in[12];
  const bf16_t* b2 = (const bf16_t*)d_in[13];
  float* out = (float*)d_out;

  char* ws = (char*)d_ws;
  bf16_t* xn = (bf16_t*)(ws);
  bf16_t* hn = (bf16_t*)(ws + (2u << 20));
  bf16_t* hbuf = (bf16_t*)(ws + (4u << 20));
  bf16_t* u = (bf16_t*)(ws + (12u << 20));
  float4* cst = (float4*)(ws + (14u << 20));
  u64* agg = (u64*)(ws + (14u << 20) + 65536);

  k1_ln<<<512, 256, 0, stream>>>(x, xn, hn, Lre, Lim, logdt, cst, (uint4*)agg);
  k2_gemm_scan<<<NB1 + NA1, 256, 0, stream>>>(xn, hn, Bre, Bim, cst, agg, hbuf, w1, b1, u);
  k3_out<<<256, 256, 0, stream>>>(hbuf, Cre, Cim, u, w2, b2, x, xn, Dsk, out);
}
```

```cpp
#include <hip/hip_runtime.h>
#include <stdint.h>

typedef unsigned short bf16_t;
typedef __attribute__((ext_vector_type(8))) short bf16x8;
typedef __attribute__((ext_vector_type(4))) short s16x4;
typedef __attribute__((ext_vector_type(16))) float f32x16;
typedef unsigned long long u64;

#define DI __device__ __forceinline__
#define GAS __attribute__((address_space(1)))
#define LAS __attribute__((address_space(3)))

constexpr int L_ = 2048;
constexpr int D_ = 512;
constexpr int P_ = 512;
constexpr int F_ = 1792;

DI float bf2f(bf16_t u) { return __uint_as_float(((unsigned)u) << 16); }
DI bf16_t f2bf(float f) { __bf16 b = (__bf16)f; return __builtin_bit_cast(bf16_t, b); }
DI float rbf(float f) { return bf2f(f2bf(f)); }

DI void glds16(const void* g, void* l) {
  __builtin_amdgcn_global_load_lds((const unsigned*)g, (unsigned*)l, 16, 0, 0);
}
#define MFMA32(a, b, c) __builtin_amdgcn_mfma_f32_32x32x16_bf16((a), (b), (c), 0, 0, 0)
#define BAR() __builtin_amdgcn_s_barrier()

template <int N> DI void wait_vm_c() { asm volatile("s_waitcnt vmcnt(%0)" ::"n"(N) : "memory"); }
template <int N> DI void wait_vm_chain(int n) {
  if constexpr (N <= 0) wait_vm_c<0>();
  else { if (n >= N) wait_vm_c<N>(); else wait_vm_chain<N - 2>(n); }
}
template <int MAXN> DI void wait_vm(int n) { wait_vm_chain<MAXN>(n); }

DI int crow(int reg, int h) { return (reg & 3) + 8 * (reg >> 2) + 4 * h; }

template <int R>
DI void stage_kc(const bf16_t* __restrict__ g, long ld, char* lds, int wave, int lane) {
  constexpr int PPW = R / 32;
#pragma unroll
  for (int i = 0; i < PPW; ++i) {
    const int piece = wave * PPW + i;
    const int row = piece * 8 + (lane >> 3);
    const int c = (lane & 7) ^ ((row >> 1) & 7);
    glds16(g + (long)row * ld + c * 8, lds + piece * 1024 + lane * 16);
  }
}
DI bf16x8 read_kc(const char* lds, int row, int ks, int h) {
  return *reinterpret_cast<const bf16x8*>(lds + row * 128 + (((2 * ks + h) ^ ((row >> 1) & 7)) << 4));
}
DI void stage_ks64(const bf16_t* __restrict__ g, long ld, char* lds, int wave, int lane) {
#pragma unroll
  for (int i = 0; i < 2; ++i) {
    const int piece = wave * 2 + i;
    const int k = piece * 8 + ((lane >> 2) & 7);
    const int col = (lane >> 5) * 32 + (lane & 3) * 8;
    glds16(g + (long)k * ld + col, lds + piece * 1024 + lane * 16);
  }
}
DI bf16x8 read_ks64(const char* lds, int cb, int ks, int lane) {
  const int h = lane >> 5, blk = (lane >> 4) & 1, q = (lane & 15) >> 2, p = lane & 3;
  const char* a = lds + (2 * ks + h) * 1024 + cb * 512 + q * 64 + blk * 32 + p * 8;
  s16x4 lo = __builtin_amdgcn_ds_read_tr16_b64_v4i16((LAS s16x4*)(a));
  s16x4 hi = __builtin_amdgcn_ds_read_tr16_b64_v4i16((LAS s16x4*)(a + 256));
  return __builtin_shufflevector(lo, hi, 0, 1, 2, 3, 4, 5, 6, 7);
}

DI float wave_sum(float v) {
#pragma unroll
  for (int o = 32; o > 0; o >>= 1) v += __shfl_xor(v, o);
  return v;
}
DI void ln_row(const float (&v)[8], float (&o)[8]) {
  float s = 0.f;
#pragma unroll
  for (int i = 0; i < 8; ++i) s += v[i];
  const float mean = wave_sum(s) * (1.0f / 512.0f);
  float q = 0.f;
#pragma unroll
  for (int i = 0; i < 8; ++i) { const float d = v[i] - mean; q += d * d; }
  const float var = wave_sum(q) * (1.0f / 512.0f);
  const float mb = rbf(mean), vb = rbf(var);
  const float den = rbf(sqrtf(rbf(vb + 1e-5f)));
#pragma unroll
  for (int i = 0; i < 8; ++i) o[i] = rbf(rbf(v[i] - mb) / den);
}

__global__ __launch_bounds__(256) void k1_ln(const bf16_t* __restrict__ x, bf16_t* __restrict__ xn, bf16_t* __restrict__ hn,
                                             const bf16_t* __restrict__ Lre, const bf16_t* __restrict__ Lim,
                                             const bf16_t* __restrict__ logdt, float4* __restrict__ cst,
                                             uint4* __restrict__ aggz) {
  const int tid = threadIdx.x, lane = tid & 63, w = tid >> 6, bid = blockIdx.x;
  if (tid < 32) aggz[bid * 32 + tid] = make_uint4(0u, 0u, 0u, 0u);
  if (bid < 2) {
    const int p = bid * 256 + tid;
    const float lre = bf2f(Lre[p]), lim = bf2f(Lim[p]);
    const float dt = rbf(expf(bf2f(logdt[p])));
    const float zr = dt * lre, zi = dt * lim;
    const float e = expf(zr);
    float sn, cs;
    sincosf(zi, &sn, &cs);
    const float br = e * cs, bi = e * sn;
    const float nr = br - 1.0f, ni = bi;
    const float den = lre * lre + lim * lim;
    cst[p] = make_float4(br, bi, (nr * lre + ni * lim) / den, (ni * lre - nr * lim) / den);
  }
  const int row = bid * 4 + w;
  const uint4 raw = reinterpret_cast<const uint4*>(x + (long)row * D_)[lane];
  const unsigned rw[4] = {raw.x, raw.y, raw.z, raw.w};
  float v[8], a[8], b[8];
#pragma unroll
  for (int i = 0; i < 4; ++i) { v[2 * i] = __uint_as_float(rw[i] << 16); v[2 * i + 1] = __uint_as_float(rw[i] & 0xffff0000u); }
  ln_row(v, a);
  ln_row(a, b);
  unsigned pa[4], pb[4];
#pragma unroll
  for (int i = 0; i < 4; ++i) {
    pa[i] = (unsigned)f2bf(a[2 * i]) | ((unsigned)f2bf(a[2 * i + 1]) << 16);
    pb[i] = (unsigned)f2bf(b[2 * i]) | ((unsigned)f2bf(b[2 * i + 1]) << 16);
  }
  reinterpret_cast<uint4*>(xn + (long)row * D_)[lane] = make_uint4(pa[0], pa[1], pa[2], pa[3]);
  reinterpret_cast<uint4*>(hn + (long)row * D_)[lane] = make_uint4(pb[0], pb[1], pb[2], pb[3]);
}

#define STAMP(i)
#define STAMP2(i)
constexpr int NB1 = 224;
constexpr int NA1 = 256;
constexpr int K2_LDS = 81920;

struct cplx { float r, i; };
DI cplx cmul(cplx a, cplx b) { return {a.r * b.r - a.i * b.i, a.r * b.i + a.i * b.r}; }
DI cplx cfma(cplx a, cplx b, cplx c) { return {c.r + a.r * b.r - a.i * b.i, c.i + a.r * b.i + a.i * b.r}; }

DI void b1_tile(int bid, const bf16_t* __restrict__ hn, const bf16_t* __restrict__ w1, const bf16_t* __restrict__ b1,
                bf16_t* __restrict__ u, char* smem) {
  const int tid = threadIdx.x, lane = tid & 63, wave = __builtin_amdgcn_readfirstlane(tid >> 6);
  const int lin = (bid & 7) * 28 + (bid >> 3);
  const int ft = lin >> 3, dt = lin & 7;
  constexpr int S = 5, NT = L_ / 64, SB = 16384;
  STAMP2(0);
  if (wave >= 4) {
    const int lw = wave - 4;
    const bf16_t* Ag = hn + dt * 64;
    const bf16_t* Bg = w1 + ft * 64;
    auto issue = [&](int t) {
      char* nb = smem + (t % S) * SB;
      stage_ks64(Ag + (long)t * 64 * D_, D_, nb, lw, lane);
      stage_ks64(Bg + (long)t * 64 * F_, F_, nb + 8192, lw, lane);
    };
#pragma unroll
    for (int s = 0; s < S - 1; ++s) issue(s);
    for (int t = 0; t < NT; ++t) {
      wait_vm<4 * (S - 2)>(4 * min(S - 2, NT - 1 - t));
      BAR();
      if (t + S - 1 < NT) issue(t + S - 1);
    }
    return;
  }
  const int wm = wave >> 1, wn = wave & 1, h = lane >> 5;
  f32x16 acc;
#pragma unroll
  for (int i = 0; i < 16; ++i) acc[i] = 0.f;
  for (int t = 0; t < NT; ++t) {
    BAR();
    const char* cb = smem + (t % S) * SB;
    bf16x8 a[4], b[4];
#pragma unroll
    for (int ks = 0; ks < 4; ++ks) {
      a[ks] = read_ks64(cb, wm, ks, lane);
      b[ks] = read_ks64(cb + 8192, wn, ks, lane);
    }
    __builtin_amdgcn_sched_barrier(0);
#pragma unroll
    for (int ks = 0; ks < 4; ++ks) acc = MFMA32(a[ks], b[ks], acc);
  }
  STAMP2(1);
  const int f = ft * 64 + wn * 32 + (lane & 31);
  const float bias = bf2f(b1[f]);
#pragma unroll
  for (int r = 0; r < 16; ++r) {
    const int d = dt * 64 + wm * 32 + crow(r, h);
    float v = rbf(acc[r]);
    v = rbf(v + bias);
    const float inner = 0.7978845608028654f * (v + 0.044715f * v * v * v);
    const float th = 1.0f - 2.0f / (__expf(2.0f * inner) + 1.0f);
    const float g = 0.5f * v * (1.0f + th);
    u[(long)d * F_ + f] = f2bf(g);
  }
  STAMP2(2);
}

DI void a1_tile(int idx, const bf16_t* __restrict__ xn, const bf16_t* __restrict__ Bre, const bf16_t* __restrict__ Bim,
                const float4* __restrict__ cst, u64* agg, bf16_t* __restrict__ hbuf, char* smem) {
  const int tid = threadIdx.x, lane = tid & 63, wave = __builtin_amdgcn_readfirstlane(tid >> 6);
  const int h = lane >> 5;
  const int tb = idx >> 3, cg = idx & 7;
  constexpr int NT = D_ / 64, SB = 24576, S = 3;
  float* X = reinterpret_cast<float*>(smem);
  float2* AG = reinterpret_cast<float2*>(smem + 32768);
  float2* E = reinterpret_cast<float2*>(smem + 73728);
  STAMP2(0);
  if (wave >= 4) {
    const int lw = wave - 4;
    const bf16_t* Ag = xn + (long)tb * 64 * D_;
    auto issue = [&](int t) {
      char* nb = smem + (t % S) * SB;
      stage_kc<64>(Ag + t * 64, D_, nb, lw, lane);
      stage_kc<64>(Bre + (long)cg * 64 * D_ + t * 64, D_, nb + 8192, lw, lane);
      stage_kc<64>(Bim + (long)cg * 64 * D_ + t * 64, D_, nb + 16384, lw, lane);
    };
    issue(0);
    issue(1);
    for (int t = 0; t < NT; ++t) {
      wait_vm<6>(t + 1 < NT ? 6 : 0);
      BAR();
      if (t + 2 < NT) issue(t + 2);
    }
    __syncthreads();
  } else {
    const int wm = wave >> 1, wn = wave & 1;
    f32x16 acc[2];
#pragma unroll
    for (int b = 0; b < 2; ++b)
#pragma unroll
      for (int i = 0; i < 16; ++i) acc[b][i] = 0.f;
#pragma unroll 1
    for (int t = 0; t < NT; ++t) {
      BAR();
      const char* cb = smem + (t % S) * SB;
      bf16x8 a[4], b[4][2];
#pragma unroll
      for (int ks = 0; ks < 4; ++ks) {
        a[ks] = read_kc(cb, wm * 32 + (lane & 31), ks, h);
#pragma unroll
        for (int n = 0; n < 2; ++n) b[ks][n] = read_kc(cb + 8192 + wn * 8192, n * 32 + (lane & 31), ks, h);
      }
      __builtin_amdgcn_sched_barrier(0);
#pragma unroll
      for (int ks = 0; ks < 4; ++ks)
#pragma unroll
        for (int n = 0; n < 2; ++n) acc[n] = MFMA32(a[ks], b[ks][n], acc[n]);
    }
    __syncthreads();
#pragma unroll
    for (int n = 0; n < 2; ++n)
#pragma unroll
      for (int r = 0; r < 16; ++r) {
        const int row = wm * 32 + crow(r, h);
        X[(wn * 64 + row) * 64 + n * 32 + (lane & 31)] = acc[n][r];
      }
  }
  __syncthreads();
  STAMP2(1);
  const int ch = lane, seg = wave;
  const float4 c4 = cst[cg * 64 + ch];
  const cplx lam = {c4.x, c4.y}, coef = {c4.z, c4.w};
  cplx g = {0.f, 0.f};
  float* Xr = X + seg * 8 * 64 + ch;
  float* Xi = Xr + 64 * 64;
#pragma unroll
  for (int j = 0; j < 8; ++j) {
    const cplx xv = {Xr[j * 64], Xi[j * 64]};
    g = cfma(lam, g, xv);
    Xr[j * 64] = g.r;
    Xi[j * 64] = g.i;
  }
  E[seg * 64 + ch] = make_float2(g.r, g.i);
  __syncthreads();
  cplx L8 = lam;
#pragma unroll
  for (int i = 0; i < 3; ++i) L8 = cmul(L8, L8);
  cplx c = {0.f, 0.f}, cin = {0.f, 0.f};
#pragma unroll
  for (int s = 0; s < 8; ++s) {
    if (s == seg) cin = c;
    const float2 e = E[s * 64 + ch];
    c = cfma(L8, c, cplx{e.x, e.y});
  }
  if (seg == 0) {
    GAS u64* ag = (GAS u64*)(agg + ((long)tb * P_ + cg * 64 + ch) * 2);
    __hip_atomic_store(ag, (1ull << 32) | (u64)__float_as_uint(c.r), __ATOMIC_RELAXED, __HIP_MEMORY_SCOPE_AGENT);
    __hip_atomic_store(ag + 1, (1ull << 32) | (u64)__float_as_uint(c.i), __ATOMIC_RELAXED, __HIP_MEMORY_SCOPE_AGENT);
  }
  cplx L64 = L8;
#pragma unroll
  for (int i = 0; i < 3; ++i) L64 = cmul(L64, L64);
  STAMP2(2);
#pragma unroll
  for (int k = 0; k < 4; ++k) {
    const int b = seg + 8 * k;
    if (b < tb) {
      GAS u64* ag = (GAS u64*)(agg + ((long)b * P_ + cg * 64 + ch) * 2);
      for (unsigned spins = 0;; ++spins) {
        const u64 vr = __hip_atomic_load(ag, __ATOMIC_RELAXED, __HIP_MEMORY_SCOPE_AGENT);
        const u64 vi = __hip_atomic_load(ag + 1, __ATOMIC_RELAXED, __HIP_MEMORY_SCOPE_AGENT);
        const bool ok = ((vr >> 32) == 1ull) && ((vi >> 32) == 1ull);
        AG[b * 64 + ch] = make_float2(__uint_as_float((unsigned)vr), __uint_as_float((unsigned)vi));
        if (__all(ok)) break;
        if (spins > 200000u) break;
        __builtin_amdgcn_s_sleep(1);
      }
    }
  }
  __syncthreads();
  cplx cb = {0.f, 0.f};
  for (int b = 0; b < tb; ++b) {
    const float2 e = AG[b * 64 + ch];
    cb = cfma(L64, cb, cplx{e.x, e.y});
  }
  STAMP2(3);
  cplx Pw = {1.f, 0.f};
  for (int s = 0; s < seg; ++s) Pw = cmul(Pw, L8);
  const cplx cs = cfma(Pw, cb, cin);
  cplx p = lam;
  bf16_t* hrow = hbuf + ((long)tb * 64 + seg * 8) * 2048 + cg * 64 + ch;
#pragma unroll
  for (int j = 0; j < 8; ++j) {
    cplx hv = {Xr[j * 64], Xi[j * 64]};
    hv = cfma(p, cs, hv);
    p = cmul(p, lam);
    hv = cmul(coef, hv);
    const float re = hv.r, nim = -hv.i;
    const bf16_t rh = f2bf(re), ih = f2bf(nim);
    bf16_t* o = hrow + (long)j * 2048;
    o[0] = rh;
    o[512] = ih;
    o[1024] = f2bf(re - bf2f(rh));
    o[1536] = f2bf(nim - bf2f(ih));
  }
  STAMP2(4);
}

__global__ __launch_bounds__(512, 4) void k2_gemm_scan(const bf16_t* __restrict__ xn, const bf16_t* __restrict__ hn,
                                                       const bf16_t* __restrict__ Bre, const bf16_t* __restrict__ Bim,
                                                       const float4* __restrict__ cst, u64* agg, bf16_t* __restrict__ hbuf,
                                                       const bf16_t* __restrict__ w1, const bf16_t* __restrict__ b1,
                                                       bf16_t* __restrict__ u) {
  __shared__ __attribute__((aligned(16))) char smem[K2_LDS];
  if (blockIdx.x < NB1) b1_tile(blockIdx.x, hn, w1, b1, u, smem);
  else a1_tile(blockIdx.x - NB1, xn, Bre, Bim, cst, agg, hbuf, smem);
}

#define ABL_KS 4
constexpr int K3_SB = 49152, K3_S = 3, K3_NLW = 8;
template <int R, int NLW>
DI void stage_kc_n(const bf16_t* __restrict__ g, long ld, char* lds, int wave, int lane) {
  constexpr int PPW = R / 8 / NLW;
#pragma unroll
  for (int i = 0; i < PPW; ++i) {
    const int piece = wave * PPW + i;
    const int row = piece * 8 + (lane >> 3);
    const int c = (lane & 7) ^ ((row >> 1) & 7);
    glds16(g + (long)row * ld + c * 8, lds + piece * 1024 + lane * 16);
  }
}
template <int NLW>
DI void stage_ks64_n(const bf16_t* __restrict__ g, long ld, char* lds, int wave, int lane) {
  constexpr int PPW = 8 / NLW;
#pragma unroll
  for (int i = 0; i < PPW; ++i) {
    const int piece = wave * PPW + i;
    const int k = piece * 8 + ((lane >> 2) & 7);
    const int col = (lane >> 5) * 32 + (lane & 3) * 8;
    glds16(g + (long)k * ld + col, lds + piece * 1024 + lane * 16);
  }
}
__global__ __launch_bounds__(768) void k3_out(const bf16_t* __restrict__ hbuf, const bf16_t* __restrict__ Cre,
                                              const bf16_t* __restrict__ Cim, const bf16_t* __restrict__ u,
                                              const bf16_t* __restrict__ w2, const bf16_t* __restrict__ b2,
                                              const bf16_t* __restrict__ x, const bf16_t* __restrict__ xn,
                                              const bf16_t* __restrict__ Dsk, float* __restrict__ out) {
  __shared__ __attribute__((aligned(16))) char smem[K3_SB * K3_S];
  const int tid = threadIdx.x, lane = tid & 63, wave = __builtin_amdgcn_readfirstlane(tid >> 6);
  const int bid = blockIdx.x;
  const int st = (bid & 7) * 4 + (bid >> 6), dt = (bid >> 3) & 7;
  constexpr int NA = 8, NB = 14, NG = NA + NB;
  STAMP(0);
  if (wave >= 4) {
    const int lw = wave - 4;
    const bf16_t* hA = hbuf + (long)st * 64 * 2048;
    auto issue = [&](int g) {
      char* nb = smem + (g % K3_S) * K3_SB;
      if (g < NA) {
#pragma unroll
        for (int hf = 0; hf < 2; ++hf) {
          const int kt = 2 * g + hf;
          stage_kc_n<64, K3_NLW>(hA + kt * 64, 2048, nb + hf * 24576, lw, lane);
          stage_kc_n<64, K3_NLW>(hA + 1024 + kt * 64, 2048, nb + hf * 24576 + 8192, lw, lane);
          stage_kc_n<64, K3_NLW>((kt < 8 ? Cre : Cim) + (long)dt * 64 * P_ + (kt & 7) * 64, P_, nb + hf * 24576 + 16384, lw, lane);
        }
      } else {
#pragma unroll
        for (int hf = 0; hf < 2; ++hf) {
          const int kt = 2 * (g - NA) + hf;
          stage_ks64_n<K3_NLW>(w2 + (long)kt * 64 * L_ + st * 64, L_, nb + hf * 16384, lw, lane);
          stage_kc_n<64, K3_NLW>(u + (long)dt * 64 * F_ + kt * 64, F_, nb + hf * 16384 + 8192, lw, lane);
        }
      }
    };
    auto gcount = [&](int g) { return g >= NG ? 0 : (g < NA ? 6 : 4); };
    issue(0);
    issue(1);
    for (int g = 0; g < NG; ++g) {
      wait_vm<6>(gcount(g + 1));
      BAR();
      if (g + K3_S - 1 < NG) issue(g + K3_S - 1);
    }
    return;
  }
  const int wm = wave >> 1, wn = wave & 1, h = lane >> 5;
  const int d = dt * 64 + wn * 32 + (lane & 31);
  unsigned xp[8], xnp[8], bp[8];
#pragma unroll
  for (int r = 0; r < 8; ++r) {
    const int s0 = st * 64 + wm * 32 + crow(2 * r, h), s1 = st * 64 + wm * 32 + crow(2 * r + 1, h);
    xp[r] = (unsigned)x[(long)s0 * D_ + d] | ((unsigned)x[(long)s1 * D_ + d] << 16);
    xnp[r] = (unsigned)xn[(long)s0 * D_ + d] | ((unsigned)xn[(long)s1 * D_ + d] << 16);
    bp[r] = (unsigned)b2[s0] | ((unsigned)b2[s1] << 16);
  }
  const float dsk = bf2f(Dsk[d]);
  f32x16 acc1, acc2;
#pragma unroll
  for (int i = 0; i < 16; ++i) { acc1[i] = 0.f; acc2[i] = 0.f; }
#pragma unroll 1
  for (int g = 0; g < NA; ++g) {
    BAR();
    const char* cb = smem + (g % K3_S) * K3_SB;
#pragma unroll
    for (int hf = 0; hf < 2; ++hf) {
      const char* c2 = cb + hf * 24576;
      bf16x8 a0[4], a1[4], b[4];
#pragma unroll
      for (int ks = 0; ks < 4; ++ks) {
        a0[ks] = read_kc(c2, wm * 32 + (lane & 31), ks, h);
        a1[ks] = read_kc(c2 + 8192, wm * 32 + (lane & 31), ks, h);
        b[ks] = read_kc(c2 + 16384, wn * 32 + (lane & 31), ks, h);
      }
      __builtin_amdgcn_sched_barrier(0);
#pragma unroll
      for (int ks = 0; ks < ABL_KS; ++ks) {
        acc1 = MFMA32(a0[ks], b[ks], acc1);
        acc1 = MFMA32(a1[ks], b[ks], acc1);
      }
      __builtin_amdgcn_sched_barrier(0);
    }
  }
  STAMP(1);
#pragma unroll 1
  for (int g = NA; g < NG; ++g) {
    BAR();
    const char* cb = smem + (g % K3_S) * K3_SB;
    bf16x8 a[8], b[8];
#pragma unroll
    for (int ks = 0; ks < 8; ++ks) {
      const char* c2 = cb + (ks >> 2) * 16384;
      a[ks] = read_ks64(c2, wm, ks & 3, lane);
      b[ks] = read_kc(c2 + 8192, wn * 32 + (lane & 31), ks & 3, h);
    }
    __builtin_amdgcn_sched_barrier(0);
#pragma unroll
    for (int ks = 0; ks < 2 * ABL_KS; ++ks) acc2 = MFMA32(a[ks], b[ks], acc2);
  }
  STAMP(2);
  int lz = lane;
  asm volatile("" : "+v"(lz));
  const int hz = lz >> 5, dz = dt * 64 + wn * 32 + (lz & 31);
#pragma unroll
  for (int r = 0; r < 16; ++r) {
    const int s = st * 64 + wm * 32 + crow(r, hz);
    const float xvr = __uint_as_float((r & 1) ? (xp[r >> 1] & 0xffff0000u) : (xp[r >> 1] << 16));
    const float xnr = __uint_as_float((r & 1) ? (xnp[r >> 1] & 0xffff0000u) : (xnp[r >> 1] << 16));
    const float bvr = __uint_as_float((r & 1) ? (bp[r >> 1] & 0xffff0000u) : (bp[r >> 1] << 16));
    const float yA = 2.0f * acc1[r] + rbf(xnr * dsk);
    float yB = rbf(acc2[r]);
    yB = rbf(yB + bvr);
    const float gg = rbf(yB / (1.0f + __expf(-yB)));
    out[(long)s * D_ + dz] = xvr + yA * gg;
  }
  STAMP(3);
}

extern "C" void kernel_launch(void* const* d_in, const int* in_sizes, int n_in, void* d_out, int out_size, void* d_ws,
                              size_t ws_size, hipStream_t stream) {
  const bf16_t* x = (const bf16_t*)d_in[0];
  const bf16_t* Lre = (const bf16_t*)d_in[1];
  const bf16_t* Lim = (const bf16_t*)d_in[2];
  const bf16_t* Bre = (const bf16_t*)d_in[3];
  const bf16_t* Bim = (const bf16_t*)d_in[4];
  const bf16_t* Cre = (const bf16_t*)d_in[5];
  const bf16_t* Cim = (const bf16_t*)d_in[6];
  const bf16_t* logdt = (const bf16_t*)d_in[7];
  const bf16_t* Dsk = (const bf16_t*)d_in[8];
  const bf16_t* w1 = (const bf16_t*)d_in[10];
  const bf16_t* b1 = (const bf16_t*)d_in[11];
  const bf16_t* w2 = (const bf16_t*)d_in[12];
  const bf16_t* b2 = (const bf16_t*)d_in[13];
  float* out = (float*)d_out;

  char* ws = (char*)d_ws;
  bf16_t* xn = (bf16_t*)(ws);
  bf16_t* hn = (bf16_t*)(ws + (2u << 20));
  bf16_t* hbuf = (bf16_t*)(ws + (4u << 20));
  bf16_t* u = (bf16_t*)(ws + (12u << 20));
  float4* cst = (float4*)(ws + (14u << 20));
  u64* agg = (u64*)(ws + (14u << 20) + 65536);

  k1_ln<<<512, 256, 0, stream>>>(x, xn, hn, Lre, Lim, logdt, cst, (uint4*)agg);
  k2_gemm_scan<<<NB1 + NA1, 512, 0, stream>>>(xn, hn, Bre, Bim, cst, agg, hbuf, w1, b1, u);
  k3_out<<<256, 768, 0, stream>>>(hbuf, Cre, Cim, u, w2, b2, x, xn, Dsk, out);
}
```

```cpp
#include <hip/hip_runtime.h>
#include <stdint.h>

typedef unsigned short bf16_t;
typedef __attribute__((ext_vector_type(8))) short bf16x8;
typedef __attribute__((ext_vector_type(4))) short s16x4;
typedef __attribute__((ext_vector_type(16))) float f32x16;
typedef unsigned long long u64;

#define DI __device__ __forceinline__
#define GAS __attribute__((address_space(1)))
#define LAS __attribute__((address_space(3)))

constexpr int L_ = 2048;
constexpr int D_ = 512;
constexpr int P_ = 512;
constexpr int F_ = 1792;

DI float bf2f(bf16_t u) { return __uint_as_float(((unsigned)u) << 16); }
DI bf16_t f2bf(float f) { __bf16 b = (__bf16)f; return __builtin_bit_cast(bf16_t, b); }
DI float rbf(float f) { return bf2f(f2bf(f)); }

DI void glds16(const void* g, void* l) {
  __builtin_amdgcn_global_load_lds((const unsigned*)g, (unsigned*)l, 16, 0, 0);
}
#define MFMA32(a, b, c) __builtin_amdgcn_mfma_f32_32x32x16_bf16((a), (b), (c), 0, 0, 0)
#define BAR() __builtin_amdgcn_s_barrier()

template <int N> DI void wait_vm_c() { asm volatile("s_waitcnt vmcnt(%0)" ::"n"(N) : "memory"); }
template <int N> DI void wait_vm_chain(int n) {
  if constexpr (N <= 0) wait_vm_c<0>();
  else { if (n >= N) wait_vm_c<N>(); else wait_vm_chain<N - 2>(n); }
}
template <int MAXN> DI void wait_vm(int n) { wait_vm_chain<MAXN>(n); }

DI int crow(int reg, int h) { return (reg & 3) + 8 * (reg >> 2) + 4 * h; }

template <int R>
DI void stage_kc(const bf16_t* __restrict__ g, long ld, char* lds, int wave, int lane) {
  constexpr int PPW = R / 32;
#pragma unroll
  for (int i = 0; i < PPW; ++i) {
    const int piece = wave * PPW + i;
    const int row = piece * 8 + (lane >> 3);
    const int c = (lane & 7) ^ ((row >> 1) & 7);
    glds16(g + (long)row * ld + c * 8, lds + piece * 1024 + lane * 16);
  }
}
DI bf16x8 read_kc(const char* lds, int row, int ks, int h) {
  return *reinterpret_cast<const bf16x8*>(lds + row * 128 + (((2 * ks + h) ^ ((row >> 1) & 7)) << 4));
}
DI void stage_ks64(const bf16_t* __restrict__ g, long ld, char* lds, int wave, int lane) {
#pragma unroll
  for (int i = 0; i < 2; ++i) {
    const int piece = wave * 2 + i;
    const int k = piece * 8 + ((lane >> 2) & 7);
    const int col = (lane >> 5) * 32 + (lane & 3) * 8;
    glds16(g + (long)k * ld + col, lds + piece * 1024 + lane * 16);
  }
}
DI bf16x8 read_ks64(const char* lds, int cb, int ks, int lane) {
  const int h = lane >> 5, blk = (lane >> 4) & 1, q = (lane & 15) >> 2, p = lane & 3;
  const char* a = lds + (2 * ks + h) * 1024 + cb * 512 + q * 64 + blk * 32 + p * 8;
  s16x4 lo = __builtin_amdgcn_ds_read_tr16_b64_v4i16((LAS s16x4*)(a));
  s16x4 hi = __builtin_amdgcn_ds_read_tr16_b64_v4i16((LAS s16x4*)(a + 256));
  return __builtin_shufflevector(lo, hi, 0, 1, 2, 3, 4, 5, 6, 7);
}

DI float wave_sum(float v) {
#pragma unroll
  for (int o = 32; o > 0; o >>= 1) v += __shfl_xor(v, o);
  return v;
}
DI void ln_row(const float (&v)[8], float (&o)[8]) {
  float s = 0.f;
#pragma unroll
  for (int i = 0; i < 8; ++i) s += v[i];
  const float mean = wave_sum(s) * (1.0f / 512.0f);
  float q = 0.f;
#pragma unroll
  for (int i = 0; i < 8; ++i) { const float d = v[i] - mean; q += d * d; }
  const float var = wave_sum(q) * (1.0f / 512.0f);
  const float mb = rbf(mean), vb = rbf(var);
  const float den = rbf(sqrtf(rbf(vb + 1e-5f)));
#pragma unroll
  for (int i = 0; i < 8; ++i) o[i] = rbf(rbf(v[i] - mb) / den);
}

__global__ __launch_bounds__(256) void k1_ln(const bf16_t* __restrict__ x, bf16_t* __restrict__ xn, bf16_t* __restrict__ hn,
                                             const bf16_t* __restrict__ Lre, const bf16_t* __restrict__ Lim,
                                             const bf16_t* __restrict__ logdt, float4* __restrict__ cst,
                                             uint4* __restrict__ aggz) {
  const int tid = threadIdx.x, lane = tid & 63, w = tid >> 6, bid = blockIdx.x;
  if (tid < 32) aggz[bid * 32 + tid] = make_uint4(0u, 0u, 0u, 0u);
  if (bid < 2) {
    const int p = bid * 256 + tid;
    const float lre = bf2f(Lre[p]), lim = bf2f(Lim[p]);
    const float dt = rbf(expf(bf2f(logdt[p])));
    const float zr = dt * lre, zi = dt * lim;
    const float e = expf(zr);
    float sn, cs;
    sincosf(zi, &sn, &cs);
    const float br = e * cs, bi = e * sn;
    const float nr = br - 1.0f, ni = bi;
    const float den = lre * lre + lim * lim;
    cst[p] = make_float4(br, bi, (nr * lre + ni * lim) / den, (ni * lre - nr * lim) / den);
  }
  const int row = bid * 4 + w;
  const uint4 raw = reinterpret_cast<const uint4*>(x + (long)row * D_)[lane];
  const unsigned rw[4] = {raw.x, raw.y, raw.z, raw.w};
  float v[8], a[8], b[8];
#pragma unroll
  for (int i = 0; i < 4; ++i) { v[2 * i] = __uint_as_float(rw[i] << 16); v[2 * i + 1] = __uint_as_float(rw[i] & 0xffff0000u); }
  ln_row(v, a);
  ln_row(a, b);
  unsigned pa[4], pb[4];
#pragma unroll
  for (int i = 0; i < 4; ++i) {
    pa[i] = (unsigned)f2bf(a[2 * i]) | ((unsigned)f2bf(a[2 * i + 1]) << 16);
    pb[i] = (unsigned)f2bf(b[2 * i]) | ((unsigned)f2bf(b[2 * i + 1]) << 16);
  }
  reinterpret_cast<uint4*>(xn + (long)row * D_)[lane] = make_uint4(pa[0], pa[1], pa[2], pa[3]);
  reinterpret_cast<uint4*>(hn + (long)row * D_)[lane] = make_uint4(pb[0], pb[1], pb[2], pb[3]);
}

constexpr int NB1 = 224;
#define STAMP(i)
#define STAMP2(i)
constexpr int NLW = 8;
template <int R>
DI void stage_kc8(const bf16_t* __restrict__ g, long ld, char* lds, int wave, int lane) {
  constexpr int PPW = R / 8 / NLW;
#pragma unroll
  for (int i = 0; i < PPW; ++i) {
    const int piece = wave * PPW + i;
    const int row = piece * 8 + (lane >> 3);
    const int c = (lane & 7) ^ ((row >> 1) & 7);
    glds16(g + (long)row * ld + c * 8, lds + piece * 1024 + lane * 16);
  }
}
DI void stage_ks8(const bf16_t* __restrict__ g, long ld, char* lds, int wave, int lane) {
  const int k = wave * 8 + ((lane >> 2) & 7);
  const int col = (lane >> 5) * 32 + (lane & 3) * 8;
  glds16(g + (long)k * ld + col, lds + wave * 1024 + lane * 16);
}

struct cplx { float r, i; };
DI cplx cmul(cplx a, cplx b) { return {a.r * b.r - a.i * b.i, a.r * b.i + a.i * b.r}; }
DI cplx cfma(cplx a, cplx b, cplx c) { return {c.r + a.r * b.r - a.i * b.i, c.i + a.r * b.i + a.i * b.r}; }

constexpr int K2_RING = 98304;
constexpr int K2_X = K2_RING;
constexpr int K2_AG = K2_X + 32768;
constexpr int K2_E = K2_AG + 16384;
constexpr int K2_LDS = K2_E + 4096;

__global__ __launch_bounds__(1024) void k2_gemm_scan(const bf16_t* __restrict__ xn, const bf16_t* __restrict__ hn,
                                                    const bf16_t* __restrict__ Bre, const bf16_t* __restrict__ Bim,
                                                    const float4* __restrict__ cst, u64* agg, bf16_t* __restrict__ hbuf,
                                                    const bf16_t* __restrict__ w1, const bf16_t* __restrict__ b1,
                                                    bf16_t* __restrict__ u) {
  __shared__ __attribute__((aligned(16))) char smem[K2_LDS];
  const int tid = threadIdx.x, lane = tid & 63, wave = __builtin_amdgcn_readfirstlane(tid >> 6);
  const int bid = blockIdx.x;
  const int tb = bid >> 3, cg = bid & 7;
  const bool hasB = bid < NB1;
  const int lin = (bid & 7) * 28 + (bid >> 3);
  const int ft = lin >> 3, dt = lin & 7;
  constexpr int NTA = D_ / 64, SA = 24576;
  constexpr int NTB = L_ / 128, SBB = 32768;
  STAMP2(0);
  if (wave >= 8) {
    const int lw = wave - 8;
    const bf16_t* Ag = xn + (long)tb * 64 * D_;
    auto issueA = [&](int t) {
      char* nb = smem + (t & 3) * SA;
      stage_kc8<64>(Ag + t * 64, D_, nb, lw, lane);
      stage_kc8<64>(Bre + (long)cg * 64 * D_ + t * 64, D_, nb + 8192, lw, lane);
      stage_kc8<64>(Bim + (long)cg * 64 * D_ + t * 64, D_, nb + 16384, lw, lane);
    };
    issueA(0); issueA(1);
    for (int t = 0; t < NTA; ++t) {
      wait_vm<3>(t + 1 < NTA ? 3 : 0);
      BAR();
      if (t + 2 < NTA) issueA(t + 2);
    }
    BAR();
    if (!hasB) return;
    const bf16_t* Hg = hn + dt * 64;
    const bf16_t* Wg = w1 + ft * 64;
    auto issueB = [&](int t) {
      char* nb = smem + (t % 3) * SBB;
#pragma unroll
      for (int hf = 0; hf < 2; ++hf) {
        stage_ks8(Hg + (long)(t * 128 + hf * 64) * D_, D_, nb + hf * 16384, lw, lane);
        stage_ks8(Wg + (long)(t * 128 + hf * 64) * F_, F_, nb + hf * 16384 + 8192, lw, lane);
      }
    };
    issueB(0); issueB(1);
    BAR();
    BAR();
    for (int t = 0; t < NTB; ++t) {
      wait_vm<4>(t + 1 < NTB ? 4 : 0);
      BAR();
      if (t + 2 < NTB) issueB(t + 2);
    }
    return;
  }
  const int h = lane >> 5;
  {
    const int wm = wave >> 2, wn = wave & 3;
    f32x16 acc;
#pragma unroll
    for (int i = 0; i < 16; ++i) acc[i] = 0.f;
#pragma unroll 1
    for (int t = 0; t < NTA; ++t) {
      BAR();
      const char* cb = smem + (t & 3) * SA;
      bf16x8 a[4], b[4];
#pragma unroll
      for (int ks = 0; ks < 4; ++ks) {
        a[ks] = read_kc(cb, wm * 32 + (lane & 31), ks, h);
        b[ks] = read_kc(cb + 8192 + (wn >> 1) * 8192, (wn & 1) * 32 + (lane & 31), ks, h);
      }
      __builtin_amdgcn_sched_barrier(0);
#pragma unroll
      for (int ks = 0; ks < 4; ++ks) acc = MFMA32(a[ks], b[ks], acc);
    }
    float* X = reinterpret_cast<float*>(smem + K2_X);
#pragma unroll
    for (int r = 0; r < 16; ++r) {
      const int row = wm * 32 + crow(r, h);
      X[((wn >> 1) * 64 + row) * 64 + (wn & 1) * 32 + (lane & 31)] = acc[r];
    }
  }
  BAR();
  const int ch = lane, seg = wave;
  const float4 c4 = cst[cg * 64 + ch];
  const cplx lam = {c4.x, c4.y}, coef = {c4.z, c4.w};
  float* Xr = reinterpret_cast<float*>(smem + K2_X) + seg * 8 * 64 + ch;
  float* Xi = Xr + 64 * 64;
  float2* E = reinterpret_cast<float2*>(smem + K2_E);
  __builtin_amdgcn_s_waitcnt(0xc07f);
  BAR();
  cplx g = {0.f, 0.f};
#pragma unroll
  for (int j = 0; j < 8; ++j) {
    const cplx xv = {Xr[j * 64], Xi[j * 64]};
    g = cfma(lam, g, xv);
    Xr[j * 64] = g.r;
    Xi[j * 64] = g.i;
  }
  E[seg * 64 + ch] = make_float2(g.r, g.i);
  __builtin_amdgcn_s_waitcnt(0xc07f);
  BAR();
  cplx L8 = lam;
#pragma unroll
  for (int i = 0; i < 3; ++i) L8 = cmul(L8, L8);
  cplx cin = {0.f, 0.f};
  {
    cplx c = {0.f, 0.f};
#pragma unroll
    for (int s = 0; s < 8; ++s) {
      if (s == seg) cin = c;
      const float2 e = E[s * 64 + ch];
      c = cfma(L8, c, cplx{e.x, e.y});
    }
    if (seg == 0) {
      GAS u64* ag = (GAS u64*)(agg + ((long)tb * P_ + cg * 64 + ch) * 2);
      __hip_atomic_store(ag, (1ull << 32) | (u64)__float_as_uint(c.r), __ATOMIC_RELAXED, __HIP_MEMORY_SCOPE_AGENT);
      __hip_atomic_store(ag + 1, (1ull << 32) | (u64)__float_as_uint(c.i), __ATOMIC_RELAXED, __HIP_MEMORY_SCOPE_AGENT);
    }
  }
  STAMP2(1);
  const int kh = wave >> 2, wm = (wave >> 1) & 1, wn = wave & 1;
  f32x16 accB;
#pragma unroll
  for (int i = 0; i < 16; ++i) accB[i] = 0.f;
  if (hasB) {
#pragma unroll 1
    for (int t = 0; t < NTB; ++t) {
      BAR();
      const char* cb = smem + (t % 3) * SBB + kh * 16384;
      bf16x8 a[4], b[4];
#pragma unroll
      for (int ks = 0; ks < 4; ++ks) {
        a[ks] = read_ks64(cb, wm, ks, lane);
        b[ks] = read_ks64(cb + 8192, wn, ks, lane);
      }
      __builtin_amdgcn_sched_barrier(0);
#pragma unroll
      for (int ks = 0; ks < 4; ++ks) accB = MFMA32(a[ks], b[ks], accB);
    }
  }
  STAMP2(2);
  float2* AG = reinterpret_cast<float2*>(smem + K2_AG);
#pragma unroll
  for (int k = 0; k < 4; ++k) {
    const int b = seg + 8 * k;
    if (b < tb) {
      GAS u64* ag = (GAS u64*)(agg + ((long)b * P_ + cg * 64 + ch) * 2);
      for (unsigned spins = 0;; ++spins) {
        const u64 vr = __hip_atomic_load(ag, __ATOMIC_RELAXED, __HIP_MEMORY_SCOPE_AGENT);
        const u64 vi = __hip_atomic_load(ag + 1, __ATOMIC_RELAXED, __HIP_MEMORY_SCOPE_AGENT);
        const bool ok = ((vr >> 32) == 1ull) && ((vi >> 32) == 1ull);
        AG[b * 64 + ch] = make_float2(__uint_as_float((unsigned)vr), __uint_as_float((unsigned)vi));
        if (__all(ok)) break;
        if (spins > 200000u) break;
        __builtin_amdgcn_s_sleep(1);
      }
    }
  }
  float* PB = reinterpret_cast<float*>(smem);
  if (hasB && kh == 1) {
#pragma unroll
    for (int r = 0; r < 16; ++r) PB[((wm * 2 + wn) * 16 + r) * 64 + lane] = accB[r];
  }
  __syncthreads();
  {
    cplx L64 = L8;
#pragma unroll
    for (int i = 0; i < 3; ++i) L64 = cmul(L64, L64);
    cplx cb = {0.f, 0.f};
    for (int b = 0; b < tb; ++b) {
      const float2 e = AG[b * 64 + ch];
      cb = cfma(L64, cb, cplx{e.x, e.y});
    }
    cplx Pw = {1.f, 0.f};
    for (int s = 0; s < seg; ++s) Pw = cmul(Pw, L8);
    const cplx cs = cfma(Pw, cb, cin);
    cplx p = lam;
    bf16_t* hrow = hbuf + ((long)tb * 64 + seg * 8) * 2048 + cg * 64 + ch;
#pragma unroll
    for (int j = 0; j < 8; ++j) {
      cplx hv = {Xr[j * 64], Xi[j * 64]};
      hv = cfma(p, cs, hv);
      p = cmul(p, lam);
      hv = cmul(coef, hv);
      const float re = hv.r, nim = -hv.i;
      const bf16_t rh = f2bf(re), ih = f2bf(nim);
      bf16_t* o = hrow + (long)j * 2048;
      o[0] = rh;
      o[512] = ih;
      o[1024] = f2bf(re - bf2f(rh));
      o[1536] = f2bf(nim - bf2f(ih));
    }
  }
  STAMP2(3);
  if (hasB && kh == 0) {
    const int f = ft * 64 + wn * 32 + (lane & 31);
    const float bias = bf2f(b1[f]);
#pragma unroll
    for (int r = 0; r < 16; ++r) {
      const int d = dt * 64 + wm * 32 + crow(r, h);
      float v = rbf(accB[r] + PB[((wm * 2 + wn) * 16 + r) * 64 + lane]);
      v = rbf(v + bias);
      const float inner = 0.7978845608028654f * (v + 0.044715f * v * v * v);
      const float th = 1.0f - 2.0f / (__expf(2.0f * inner) + 1.0f);
      u[(long)d * F_ + f] = f2bf(0.5f * v * (1.0f + th));
    }
  }
  STAMP2(4);
}

constexpr int K3_SB = 49152, K3_S = 3;
__global__ __launch_bounds__(1024) void k3_out(const bf16_t* __restrict__ hbuf, const bf16_t* __restrict__ Cre,
                                               const bf16_t* __restrict__ Cim, const bf16_t* __restrict__ u,
                                               const bf16_t* __restrict__ w2, const bf16_t* __restrict__ b2,
                                               const bf16_t* __restrict__ x, const bf16_t* __restrict__ xn,
                                               const bf16_t* __restrict__ Dsk, float* __restrict__ out) {
  __shared__ __attribute__((aligned(16))) char smem[K3_SB * K3_S];
  const int tid = threadIdx.x, lane = tid & 63, wave = __builtin_amdgcn_readfirstlane(tid >> 6);
  const int bid = blockIdx.x;
  const int st = (bid & 7) * 4 + (bid >> 6), dt = (bid >> 3) & 7;
  constexpr int NA = 8, NB = 14, NG = NA + NB;
  STAMP(0);
  if (wave >= 8) {
    const int lw = wave - 8;
    const bf16_t* hA = hbuf + (long)st * 64 * 2048;
    auto issue = [&](int g) {
      char* nb = smem + (g % K3_S) * K3_SB;
      if (g < NA) {
#pragma unroll
        for (int hf = 0; hf < 2; ++hf) {
          const int kt = 2 * g + hf;
          stage_kc8<64>(hA + kt * 64, 2048, nb + hf * 24576, lw, lane);
          stage_kc8<64>(hA + 1024 + kt * 64, 2048, nb + hf * 24576 + 8192, lw, lane);
          stage_kc8<64>((kt < 8 ? Cre : Cim) + (long)dt * 64 * P_ + (kt & 7) * 64, P_, nb + hf * 24576 + 16384, lw, lane);
        }
      } else {
#pragma unroll
        for (int hf = 0; hf < 2; ++hf) {
          const int kt = 2 * (g - NA) + hf;
          stage_ks8(w2 + (long)kt * 64 * L_ + st * 64, L_, nb + hf * 16384, lw, lane);
          stage_kc8<64>(u + (long)dt * 64 * F_ + kt * 64, F_, nb + hf * 16384 + 8192, lw, lane);
        }
      }
    };
    auto gcount = [&](int g) { return g >= NG ? 0 : (g < NA ? 6 : 4); };
    issue(0);
    issue(1);
    for (int g = 0; g < NG; ++g) {
      wait_vm<6>(gcount(g + 1));
      BAR();
      if (g + K3_S - 1 < NG) issue(g + K3_S - 1);
    }
    return;
  }
  const int kh = wave >> 2, wm = (wave >> 1) & 1, wn = wave & 1, h = lane >> 5;
  f32x16 acc1, acc2;
#pragma unroll
  for (int i = 0; i < 16; ++i) { acc1[i] = 0.f; acc2[i] = 0.f; }
#pragma unroll 1
  for (int g = 0; g < NA; ++g) {
    BAR();
    const char* c2 = smem + (g % K3_S) * K3_SB + kh * 24576;
    bf16x8 a0[4], a1[4], b[4];
#pragma unroll
    for (int ks = 0; ks < 4; ++ks) {
      a0[ks] = read_kc(c2, wm * 32 + (lane & 31), ks, h);
      a1[ks] = read_kc(c2 + 8192, wm * 32 + (lane & 31), ks, h);
      b[ks] = read_kc(c2 + 16384, wn * 32 + (lane & 31), ks, h);
    }
    __builtin_amdgcn_sched_barrier(0);
#pragma unroll
    for (int ks = 0; ks < 4; ++ks) {
      acc1 = MFMA32(a0[ks], b[ks], acc1);
      acc1 = MFMA32(a1[ks], b[ks], acc1);
    }
  }
  STAMP(1);
  unsigned xp[8], xnp[8], bp[8];
  {
    const int d = dt * 64 + wn * 32 + (lane & 31);
#pragma unroll
    for (int r = 0; r < 8; ++r) {
      const int s0 = st * 64 + wm * 32 + crow(2 * r, h), s1 = st * 64 + wm * 32 + crow(2 * r + 1, h);
      xp[r] = (unsigned)x[(long)s0 * D_ + d] | ((unsigned)x[(long)s1 * D_ + d] << 16);
      xnp[r] = (unsigned)xn[(long)s0 * D_ + d] | ((unsigned)xn[(long)s1 * D_ + d] << 16);
      bp[r] = (unsigned)b2[s0] | ((unsigned)b2[s1] << 16);
    }
  }
#pragma unroll 1
  for (int g = NA; g < NG; ++g) {
    BAR();
    const char* c2 = smem + (g % K3_S) * K3_SB + kh * 16384;
    bf16x8 a[4], b[4];
#pragma unroll
    for (int ks = 0; ks < 4; ++ks) {
      a[ks] = read_ks64(c2, wm, ks, lane);
      b[ks] = read_kc(c2 + 8192, wn * 32 + (lane & 31), ks, h);
    }
    __builtin_amdgcn_sched_barrier(0);
#pragma unroll
    for (int ks = 0; ks < 4; ++ks) acc2 = MFMA32(a[ks], b[ks], acc2);
  }
  STAMP(2);
  __syncthreads();
  float* PB = reinterpret_cast<float*>(smem);
  if (kh == 1) {
#pragma unroll
    for (int r = 0; r < 16; ++r) {
      PB[((wm * 2 + wn) * 32 + r) * 64 + lane] = acc1[r];
      PB[((wm * 2 + wn) * 32 + 16 + r) * 64 + lane] = acc2[r];
    }
  }
  __syncthreads();
  if (kh == 1) return;
  int lz = lane;
  asm volatile("" : "+v"(lz));
  const int hz = lz >> 5, dz = dt * 64 + wn * 32 + (lz & 31);
  const float dsk = bf2f(Dsk[dz]);
#pragma unroll
  for (int r = 0; r < 16; ++r) {
    const int s = st * 64 + wm * 32 + crow(r, hz);
    const float xvr = __uint_as_float((r & 1) ? (xp[r >> 1] & 0xffff0000u) : (xp[r >> 1] << 16));
    const float xnr = __uint_as_float((r & 1) ? (xnp[r >> 1] & 0xffff0000u) : (xnp[r >> 1] << 16));
    const float bvr = __uint_as_float((r & 1) ? (bp[r >> 1] & 0xffff0000u) : (bp[r >> 1] << 16));
    const float a1v = acc1[r] + PB[((wm * 2 + wn) * 32 + r) * 64 + lz];
    const float a2v = acc2[r] + PB[((wm * 2 + wn) * 32 + 16 + r) * 64 + lz];
    const float yA = 2.0f * a1v + rbf(xnr * dsk);
    float yB = rbf(a2v);
    yB = rbf(yB + bvr);
    const float gg = rbf(yB / (1.0f + __expf(-yB)));
    out[(long)s * D_ + dz] = xvr + yA * gg;
  }
  STAMP(3);
}

extern "C" void kernel_launch(void* const* d_in, const int* in_sizes, int n_in, void* d_out, int out_size, void* d_ws,
                              size_t ws_size, hipStream_t stream) {
  const bf16_t* x = (const bf16_t*)d_in[0];
  const bf16_t* Lre = (const bf16_t*)d_in[1];
  const bf16_t* Lim = (const bf16_t*)d_in[2];
  const bf16_t* Bre = (const bf16_t*)d_in[3];
  const bf16_t* Bim = (const bf16_t*)d_in[4];
  const bf16_t* Cre = (const bf16_t*)d_in[5];
  const bf16_t* Cim = (const bf16_t*)d_in[6];
  const bf16_t* logdt = (const bf16_t*)d_in[7];
  const bf16_t* Dsk = (const bf16_t*)d_in[8];
  const bf16_t* w1 = (const bf16_t*)d_in[10];
  const bf16_t* b1 = (const bf16_t*)d_in[11];
  const bf16_t* w2 = (const bf16_t*)d_in[12];
  const bf16_t* b2 = (const bf16_t*)d_in[13];
  float* out = (float*)d_out;

  char* ws = (char*)d_ws;
  bf16_t* xn = (bf16_t*)(ws);
  bf16_t* hn = (bf16_t*)(ws + (2u << 20));
  bf16_t* hbuf = (bf16_t*)(ws + (4u << 20));
  bf16_t* u = (bf16_t*)(ws + (12u << 20));
  float4* cst = (float4*)(ws + (14u << 20));
  u64* agg = (u64*)(ws + (14u << 20) + 65536);

  k1_ln<<<512, 256, 0, stream>>>(x, xn, hn, Lre, Lim, logdt, cst, (uint4*)agg);
  k2_gemm_scan<<<256, 1024, 0, stream>>>(xn, hn, Bre, Bim, cst, agg, hbuf, w1, b1, u);
  k3_out<<<256, 1024, 0, stream>>>(hbuf, Cre, Cim, u, w2, b2, x, xn, Dsk, out);
}
```

```cpp
#include <hip/hip_runtime.h>
#include <stdint.h>

typedef unsigned short bf16_t;
typedef __attribute__((ext_vector_type(8))) short bf16x8;
typedef __attribute__((ext_vector_type(4))) short s16x4;
typedef __attribute__((ext_vector_type(16))) float f32x16;
typedef unsigned long long u64;

#define DI __device__ __forceinline__
#define GAS __attribute__((address_space(1)))
#define LAS __attribute__((address_space(3)))

constexpr int L_ = 2048;
constexpr int D_ = 512;
constexpr int P_ = 512;
constexpr int F_ = 1792;

DI float bf2f(bf16_t u) { return __uint_as_float(((unsigned)u) << 16); }
DI bf16_t f2bf(float f) { __bf16 b = (__bf16)f; return __builtin_bit_cast(bf16_t, b); }
DI float rbf(float f) { return bf2f(f2bf(f)); }

DI void glds16(const void* g, void* l) {
  __builtin_amdgcn_global_load_lds((const unsigned*)g, (unsigned*)l, 16, 0, 0);
}
#define MFMA32(a, b, c) __builtin_amdgcn_mfma_f32_32x32x16_bf16((a), (b), (c), 0, 0, 0)
#define BAR() __builtin_amdgcn_s_barrier()

template <int N> DI void wait_vm_c() { asm volatile("s_waitcnt vmcnt(%0)" ::"n"(N) : "memory"); }
template <int N> DI void wait_vm_chain(int n) {
  if constexpr (N <= 0) wait_vm_c<0>();
  else { if (n >= N) wait_vm_c<N>(); else wait_vm_chain<N - 2>(n); }
}
template <int MAXN> DI void wait_vm(int n) { wait_vm_chain<MAXN>(n); }

DI int crow(int reg, int h) { return (reg & 3) + 8 * (reg >> 2) + 4 * h; }

template <int R>
DI void stage_kc(const bf16_t* __restrict__ g, long ld, char* lds, int wave, int lane) {
  constexpr int PPW = R / 32;
#pragma unroll
  for (int i = 0; i < PPW; ++i) {
    const int piece = wave * PPW + i;
    const int row = piece * 8 + (lane >> 3);
    const int c = (lane & 7) ^ ((row >> 1) & 7);
    glds16(g + (long)row * ld + c * 8, lds + piece * 1024 + lane * 16);
  }
}
DI bf16x8 read_kc(const char* lds, int row, int ks, int h) {
  return *reinterpret_cast<const bf16x8*>(lds + row * 128 + (((2 * ks + h) ^ ((row >> 1) & 7)) << 4));
}
DI void stage_ks64(const bf16_t* __restrict__ g, long ld, char* lds, int wave, int lane) {
#pragma unroll
  for (int i = 0; i < 2; ++i) {
    const int piece = wave * 2 + i;
    const int k = piece * 8 + ((lane >> 2) & 7);
    const int col = (lane >> 5) * 32 + (lane & 3) * 8;
    glds16(g + (long)k * ld + col, lds + piece * 1024 + lane * 16);
  }
}
DI bf16x8 read_ks64(const char* lds, int cb, int ks, int lane) {
  const int h = lane >> 5, blk = (lane >> 4) & 1, q = (lane & 15) >> 2, p = lane & 3;
  const char* a = lds + (2 * ks + h) * 1024 + cb * 512 + q * 64 + blk * 32 + p * 8;
  s16x4 lo = __builtin_amdgcn_ds_read_tr16_b64_v4i16((LAS s16x4*)(a));
  s16x4 hi = __builtin_amdgcn_ds_read_tr16_b64_v4i16((LAS s16x4*)(a + 256));
  return __builtin_shufflevector(lo, hi, 0, 1, 2, 3, 4, 5, 6, 7);
}

template <int CTRL, int RMASK> DI float dpp_add(float v) {
  const int t = __builtin_amdgcn_update_dpp(0, __float_as_int(v), CTRL, RMASK, 0xf, true);
  return v + __int_as_float(t);
}
DI float wave_sum(float v) {
  v = dpp_add<0x111, 0xf>(v);
  v = dpp_add<0x112, 0xf>(v);
  v = dpp_add<0x114, 0xf>(v);
  v = dpp_add<0x118, 0xf>(v);
  v = dpp_add<0x142, 0xa>(v);
  v = dpp_add<0x143, 0xc>(v);
  return __int_as_float(__builtin_amdgcn_readlane(__float_as_int(v), 63));
}
DI void ln_row(const float (&v)[8], float (&o)[8]) {
  float s = 0.f;
#pragma unroll
  for (int i = 0; i < 8; ++i) s += v[i];
  const float mean = wave_sum(s) * (1.0f / 512.0f);
  float q = 0.f;
#pragma unroll
  for (int i = 0; i < 8; ++i) { const float d = v[i] - mean; q += d * d; }
  const float var = wave_sum(q) * (1.0f / 512.0f);
  const float mb = rbf(mean), vb = rbf(var);
  const float den = rbf(sqrtf(rbf(vb + 1e-5f)));
#pragma unroll
  for (int i = 0; i < 8; ++i) o[i] = rbf(rbf(v[i] - mb) / den);
}

__global__ __launch_bounds__(256) void k1_ln(const bf16_t* __restrict__ x, bf16_t* __restrict__ xn, bf16_t* __restrict__ hn,
                                             const bf16_t* __restrict__ Lre, const bf16_t* __restrict__ Lim,
                                             const bf16_t* __restrict__ logdt, float4* __restrict__ cst,
                                             uint4* __restrict__ aggz) {
  const int tid = threadIdx.x, lane = tid & 63, w = tid >> 6, bid = blockIdx.x;
  if (tid < 32) aggz[bid * 32 + tid] = make_uint4(0u, 0u, 0u, 0u);
  if (bid < 2) {
    const int p = bid * 256 + tid;
    const float lre = bf2f(Lre[p]), lim = bf2f(Lim[p]);
    const float dt = rbf(expf(bf2f(logdt[p])));
    const float zr = dt * lre, zi = dt * lim;
    const float e = expf(zr);
    float sn, cs;
    sincosf(zi, &sn, &cs);
    const float br = e * cs, bi = e * sn;
    const float nr = br - 1.0f, ni = bi;
    const float den = lre * lre + lim * lim;
    cst[p] = make_float4(br, bi, (nr * lre + ni * lim) / den, (ni * lre - nr * lim) / den);
  }
  const int row = bid * 4 + w;
  const uint4 raw = reinterpret_cast<const uint4*>(x + (long)row * D_)[lane];
  const unsigned rw[4] = {raw.x, raw.y, raw.z, raw.w};
  float v[8], a[8], b[8];
#pragma unroll
  for (int i = 0; i < 4; ++i) { v[2 * i] = __uint_as_float(rw[i] << 16); v[2 * i + 1] = __uint_as_float(rw[i] & 0xffff0000u); }
  ln_row(v, a);
  ln_row(a, b);
  unsigned pa[4], pb[4];
#pragma unroll
  for (int i = 0; i < 4; ++i) {
    pa[i] = (unsigned)f2bf(a[2 * i]) | ((unsigned)f2bf(a[2 * i + 1]) << 16);
    pb[i] = (unsigned)f2bf(b[2 * i]) | ((unsigned)f2bf(b[2 * i + 1]) << 16);
  }
  reinterpret_cast<uint4*>(xn + (long)row * D_)[lane] = make_uint4(pa[0], pa[1], pa[2], pa[3]);
  reinterpret_cast<uint4*>(hn + (long)row * D_)[lane] = make_uint4(pb[0], pb[1], pb[2], pb[3]);
}

constexpr int NB1 = 224;
#define STAMP(i)
#define STAMP2(i)
constexpr int NLW = 8;
template <int R>
DI void stage_kc8(const bf16_t* __restrict__ g, long ld, char* lds, int wave, int lane) {
  constexpr int PPW = R / 8 / NLW;
#pragma unroll
  for (int i = 0; i < PPW; ++i) {
    const int piece = wave * PPW + i;
    const int row = piece * 8 + (lane >> 3);
    const int c = (lane & 7) ^ ((row >> 1) & 7);
    glds16(g + (long)row * ld + c * 8, lds + piece * 1024 + lane * 16);
  }
}
DI void stage_ks8(const bf16_t* __restrict__ g, long ld, char* lds, int wave, int lane) {
  const int k = wave * 8 + ((lane >> 2) & 7);
  const int col = (lane >> 5) * 32 + (lane & 3) * 8;
  glds16(g + (long)k * ld + col, lds + wave * 1024 + lane * 16);
}

struct cplx { float r, i; };
DI cplx cmul(cplx a, cplx b) { return {a.r * b.r - a.i * b.i, a.r * b.i + a.i * b.r}; }
DI cplx cfma(cplx a, cplx b, cplx c) { return {c.r + a.r * b.r - a.i * b.i, c.i + a.r * b.i + a.i * b.r}; }

constexpr int K2_RING = 98304;
constexpr int K2_X = K2_RING;
constexpr int K2_AG = K2_X + 32768;
constexpr int K2_E = K2_AG + 16384;
constexpr int K2_CNT = K2_E + 4096;
constexpr int K2_LDS = K2_CNT + 16;

__global__ __launch_bounds__(768) void k2_gemm_scan(const bf16_t* __restrict__ xn, const bf16_t* __restrict__ hn,
                                                   const bf16_t* __restrict__ Bre, const bf16_t* __restrict__ Bim,
                                                   const float4* __restrict__ cst, u64* agg, bf16_t* __restrict__ hbuf,
                                                   const bf16_t* __restrict__ w1, const bf16_t* __restrict__ b1,
                                                   bf16_t* __restrict__ u) {
  __shared__ __attribute__((aligned(16))) char smem[K2_LDS];
  const int tid = threadIdx.x, lane = tid & 63, wave = __builtin_amdgcn_readfirstlane(tid >> 6);
  const int bid = blockIdx.x;
  const int tb = bid >> 3, cg = bid & 7;
  const bool hasB = bid < NB1;
  const int lin = (bid & 7) * 28 + (bid >> 3);
  const int ft = lin >> 3, dt = lin & 7;
  constexpr int NTA = D_ / 64, SA = 24576;
  constexpr int NTB = L_ / 128, SBB = 32768;
  STAMP2(0);
  if (tid == 0) *reinterpret_cast<unsigned*>(smem + K2_CNT) = 0u;
  if (wave >= 4) {
    const int lw = wave - 4;
    const bf16_t* Ag = xn + (long)tb * 64 * D_;
    auto issueA = [&](int t) {
      char* nb = smem + (t & 3) * SA;
      stage_kc8<64>(Ag + t * 64, D_, nb, lw, lane);
      stage_kc8<64>(Bre + (long)cg * 64 * D_ + t * 64, D_, nb + 8192, lw, lane);
      stage_kc8<64>(Bim + (long)cg * 64 * D_ + t * 64, D_, nb + 16384, lw, lane);
    };
    issueA(0); issueA(1); issueA(2);
    for (int t = 0; t < NTA; ++t) {
      wait_vm<6>(3 * min(2, NTA - 1 - t));
      BAR();
      if (t + 3 < NTA) issueA(t + 3);
    }
    BAR();
    if (hasB) {
      const bf16_t* Hg = hn + dt * 64;
      const bf16_t* Wg = w1 + ft * 64;
      auto issueB = [&](int t) {
        char* nb = smem + (t % 3) * SBB;
#pragma unroll
        for (int hf = 0; hf < 2; ++hf) {
          stage_ks8(Hg + (long)(t * 128 + hf * 64) * D_, D_, nb + hf * 16384, lw, lane);
          stage_ks8(Wg + (long)(t * 128 + hf * 64) * F_, F_, nb + hf * 16384 + 8192, lw, lane);
        }
      };
      issueB(0); issueB(1);
      BAR();
      for (int t = 0; t < NTB; ++t) {
        wait_vm<4>(t + 1 < NTB ? 4 : 0);
        BAR();
        if (t + 2 < NTB) issueB(t + 2);
      }
    } else {
      BAR();
    }
    const int ch = lane;
    const float4 c4 = cst[cg * 64 + ch];
    const cplx lam = {c4.x, c4.y}, coef = {c4.z, c4.w};
    float2* AG = reinterpret_cast<float2*>(smem + K2_AG);
    const float2* E = reinterpret_cast<const float2*>(smem + K2_E);
    for (unsigned spins = 0;; ++spins) {
      u64 vr[4], vi[4];
#pragma unroll
      for (int k = 0; k < 4; ++k) {
        const int b = lw + 8 * k;
        GAS u64* ag = (GAS u64*)(agg + ((long)(b < tb ? b : 0) * P_ + cg * 64 + ch) * 2);
        vr[k] = __hip_atomic_load(ag, __ATOMIC_RELAXED, __HIP_MEMORY_SCOPE_AGENT);
        vi[k] = __hip_atomic_load(ag + 1, __ATOMIC_RELAXED, __HIP_MEMORY_SCOPE_AGENT);
      }
      bool ok = true;
#pragma unroll
      for (int k = 0; k < 4; ++k) {
        const int b = lw + 8 * k;
        if (b < tb) {
          ok = ok && ((vr[k] >> 32) == 1ull) && ((vi[k] >> 32) == 1ull);
          AG[b * 64 + ch] = make_float2(__uint_as_float((unsigned)vr[k]), __uint_as_float((unsigned)vi[k]));
        }
      }
      if (__all(ok)) break;
      if (spins > 200000u) break;
      __builtin_amdgcn_s_sleep(1);
    }
    __builtin_amdgcn_s_waitcnt(0xc07f);
    unsigned* cnt = reinterpret_cast<unsigned*>(smem + K2_CNT);
    if (lane == 0) atomicAdd(cnt, 1u);
    for (unsigned spins = 0; spins < 4000000u; ++spins) {
      if (*(volatile unsigned*)cnt >= 8u) break;
    }
    cplx L8 = lam;
#pragma unroll
    for (int i = 0; i < 3; ++i) L8 = cmul(L8, L8);
    const cplx L16 = cmul(L8, L8);
    cplx L64 = cmul(L16, L16);
    L64 = cmul(L64, L64);
    const int s16 = lw >> 1;
    cplx cin = {0.f, 0.f};
#pragma unroll
    for (int s = 0; s < 4; ++s) {
      if (s < s16) { const float2 e = E[s * 64 + ch]; cin = cfma(L16, cin, cplx{e.x, e.y}); }
    }
    cplx cb = {0.f, 0.f};
    for (int b = 0; b < tb; ++b) {
      const float2 e = AG[b * 64 + ch];
      cb = cfma(L64, cb, cplx{e.x, e.y});
    }
    cplx Pw = {1.f, 0.f};
    for (int s = 0; s < s16; ++s) Pw = cmul(Pw, L16);
    const cplx cs = cfma(Pw, cb, cin);
    cplx p = (lw & 1) ? cmul(L8, lam) : lam;
    const float* Xr = reinterpret_cast<const float*>(smem + K2_X) + lw * 8 * 64 + ch;
    const float* Xi = Xr + 64 * 64;
    bf16_t* hrow = hbuf + ((long)tb * 64 + lw * 8) * 2048 + cg * 64 + ch;
#pragma unroll
    for (int j = 0; j < 8; ++j) {
      cplx hv = {Xr[j * 64], Xi[j * 64]};
      hv = cfma(p, cs, hv);
      p = cmul(p, lam);
      hv = cmul(coef, hv);
      const float re = hv.r, nim = -hv.i;
      const bf16_t rh = f2bf(re), ih = f2bf(nim);
      bf16_t* o = hrow + (long)j * 2048;
      o[0] = rh;
      o[512] = ih;
      o[1024] = f2bf(re - bf2f(rh));
      o[1536] = f2bf(nim - bf2f(ih));
    }
    return;
  }
  const int h = lane >> 5;
  const int wm = wave >> 1, wn = wave & 1;
  {
    f32x16 acc[2];
#pragma unroll
    for (int i = 0; i < 16; ++i) { acc[0][i] = 0.f; acc[1][i] = 0.f; }
#pragma unroll 1
    for (int t = 0; t < NTA; ++t) {
      BAR();
      const char* cb = smem + (t & 3) * SA;
      bf16x8 a[4], b[4][2];
#pragma unroll
      for (int ks = 0; ks < 4; ++ks) {
        a[ks] = read_kc(cb, wm * 32 + (lane & 31), ks, h);
#pragma unroll
        for (int n = 0; n < 2; ++n) b[ks][n] = read_kc(cb + 8192 + wn * 8192, n * 32 + (lane & 31), ks, h);
      }
      __builtin_amdgcn_sched_barrier(0);
#pragma unroll
      for (int ks = 0; ks < 4; ++ks)
#pragma unroll
        for (int n = 0; n < 2; ++n) acc[n] = MFMA32(a[ks], b[ks][n], acc[n]);
    }
    float* X = reinterpret_cast<float*>(smem + K2_X);
#pragma unroll
    for (int n = 0; n < 2; ++n)
#pragma unroll
      for (int r = 0; r < 16; ++r) {
        const int row = wm * 32 + crow(r, h);
        X[(wn * 64 + row) * 64 + n * 32 + (lane & 31)] = acc[n][r];
      }
  }
  __builtin_amdgcn_s_waitcnt(0xc07f);
  BAR();
  const int ch = lane, seg = wave;
  const float4 c4 = cst[cg * 64 + ch];
  const cplx lam = {c4.x, c4.y}, coef = {c4.z, c4.w};
  float* Xr = reinterpret_cast<float*>(smem + K2_X) + seg * 16 * 64 + ch;
  float* Xi = Xr + 64 * 64;
  float2* E = reinterpret_cast<float2*>(smem + K2_E);
  {
    cplx g = {0.f, 0.f};
#pragma unroll
    for (int j = 0; j < 16; ++j) {
      const cplx xv = {Xr[j * 64], Xi[j * 64]};
      g = cfma(lam, g, xv);
      Xr[j * 64] = g.r;
      Xi[j * 64] = g.i;
    }
    E[seg * 64 + ch] = make_float2(g.r, g.i);
  }
  __builtin_amdgcn_s_waitcnt(0xc07f);
  BAR();
  cplx L16 = lam;
#pragma unroll
  for (int i = 0; i < 4; ++i) L16 = cmul(L16, L16);
  cplx cin = {0.f, 0.f};
  {
    cplx c = {0.f, 0.f};
#pragma unroll
    for (int s = 0; s < 4; ++s) {
      if (s == seg) cin = c;
      const float2 e = E[s * 64 + ch];
      c = cfma(L16, c, cplx{e.x, e.y});
    }
    if (seg == 0) {
      GAS u64* ag = (GAS u64*)(agg + ((long)tb * P_ + cg * 64 + ch) * 2);
      __hip_atomic_store(ag, (1ull << 32) | (u64)__float_as_uint(c.r), __ATOMIC_RELAXED, __HIP_MEMORY_SCOPE_AGENT);
      __hip_atomic_store(ag + 1, (1ull << 32) | (u64)__float_as_uint(c.i), __ATOMIC_RELAXED, __HIP_MEMORY_SCOPE_AGENT);
    }
  }
  STAMP2(1);
  f32x16 accB;
#pragma unroll
  for (int i = 0; i < 16; ++i) accB[i] = 0.f;
  if (hasB) {
#pragma unroll 1
    for (int t = 0; t < NTB; ++t) {
      BAR();
      const char* cb = smem + (t % 3) * SBB;
      bf16x8 a[8], b[8];
#pragma unroll
      for (int ks = 0; ks < 8; ++ks) {
        a[ks] = read_ks64(cb + (ks >> 2) * 16384, wm, ks & 3, lane);
        b[ks] = read_ks64(cb + (ks >> 2) * 16384 + 8192, wn, ks & 3, lane);
      }
      __builtin_amdgcn_sched_barrier(0);
#pragma unroll
      for (int ks = 0; ks < 8; ++ks) accB = MFMA32(a[ks], b[ks], accB);
    }
  }
  STAMP2(2);
  STAMP2(3);
  if (hasB) {
    const int f = ft * 64 + wn * 32 + (lane & 31);
    const float bias = bf2f(b1[f]);
#pragma unroll
    for (int r = 0; r < 16; ++r) {
      const int d = dt * 64 + wm * 32 + crow(r, h);
      float v = rbf(accB[r]);
      v = rbf(v + bias);
      const float inner = 0.7978845608028654f * (v + 0.044715f * v * v * v);
      const float th = 1.0f - 2.0f / (__expf(2.0f * inner) + 1.0f);
      u[(long)d * F_ + f] = f2bf(0.5f * v * (1.0f + th));
    }
  }
  STAMP2(4);
}

constexpr int K3_SB = 49152, K3_S = 3;
__global__ __launch_bounds__(768) void k3_out(const bf16_t* __restrict__ hbuf, const bf16_t* __restrict__ Cre,
                                              const bf16_t* __restrict__ Cim, const bf16_t* __restrict__ u,
                                              const bf16_t* __restrict__ w2, const bf16_t* __restrict__ b2,
                                              const bf16_t* __restrict__ x, const bf16_t* __restrict__ xn,
                                              const bf16_t* __restrict__ Dsk, float* __restrict__ out) {
  __shared__ __attribute__((aligned(16))) char smem[K3_SB * K3_S];
  const int tid = threadIdx.x, lane = tid & 63, wave = __builtin_amdgcn_readfirstlane(tid >> 6);
  const int bid = blockIdx.x;
  const int st = (bid & 7) * 4 + (bid >> 6), dt = (bid >> 3) & 7;
  constexpr int NA = 8, NB = 14, NG = NA + NB;
  STAMP(0);
  if (wave >= 4) {
    const int lw = wave - 4;
    const bf16_t* hA = hbuf + (long)st * 64 * 2048;
    auto issue = [&](int g) {
      char* nb = smem + (g % K3_S) * K3_SB;
      if (g < NA) {
#pragma unroll
        for (int hf = 0; hf < 2; ++hf) {
          const int kt = 2 * g + hf;
          stage_kc8<64>(hA + kt * 64, 2048, nb + hf * 24576, lw, lane);
          stage_kc8<64>(hA + 1024 + kt * 64, 2048, nb + hf * 24576 + 8192, lw, lane);
          stage_kc8<64>((kt < 8 ? Cre : Cim) + (long)dt * 64 * P_ + (kt & 7) * 64, P_, nb + hf * 24576 + 16384, lw, lane);
        }
      } else {
#pragma unroll
        for (int hf = 0; hf < 2; ++hf) {
          const int kt = 2 * (g - NA) + hf;
          stage_ks8(w2 + (long)kt * 64 * L_ + st * 64, L_, nb + hf * 16384, lw, lane);
          stage_kc8<64>(u + (long)dt * 64 * F_ + kt * 64, F_, nb + hf * 16384 + 8192, lw, lane);
        }
      }
    };
    auto gcount = [&](int g) { return g >= NG ? 0 : (g < NA ? 6 : 4); };
    issue(0);
    issue(1);
    for (int g = 0; g < NG; ++g) {
      wait_vm<6>(gcount(g + 1));
      BAR();
      if (g + K3_S - 1 < NG) issue(g + K3_S - 1);
    }
    return;
  }
  const int wm = wave >> 1, wn = wave & 1, h = lane >> 5;
  f32x16 acc1, acc2;
#pragma unroll
  for (int i = 0; i < 16; ++i) { acc1[i] = 0.f; acc2[i] = 0.f; }
#pragma unroll 1
  for (int g = 0; g < NA; ++g) {
    BAR();
    const char* cb = smem + (g % K3_S) * K3_SB;
#pragma unroll
    for (int hf = 0; hf < 2; ++hf) {
      const char* c2 = cb + hf * 24576;
      bf16x8 a0[4], a1[4], b[4];
#pragma unroll
      for (int ks = 0; ks < 4; ++ks) {
        a0[ks] = read_kc(c2, wm * 32 + (lane & 31), ks, h);
        a1[ks] = read_kc(c2 + 8192, wm * 32 + (lane & 31), ks, h);
        b[ks] = read_kc(c2 + 16384, wn * 32 + (lane & 31), ks, h);
      }
      __builtin_amdgcn_sched_barrier(0);
#pragma unroll
      for (int ks = 0; ks < 4; ++ks) {
        acc1 = MFMA32(a0[ks], b[ks], acc1);
        acc1 = MFMA32(a1[ks], b[ks], acc1);
      }
      __builtin_amdgcn_sched_barrier(0);
    }
  }
  STAMP(1);
  unsigned xp[8], xnp[8], bp[8];
  {
    const int d = dt * 64 + wn * 32 + (lane & 31);
#pragma unroll
    for (int r = 0; r < 8; ++r) {
      const int s0 = st * 64 + wm * 32 + crow(2 * r, h), s1 = st * 64 + wm * 32 + crow(2 * r + 1, h);
      xp[r] = (unsigned)x[(long)s0 * D_ + d] | ((unsigned)x[(long)s1 * D_ + d] << 16);
      xnp[r] = (unsigned)xn[(long)s0 * D_ + d] | ((unsigned)xn[(long)s1 * D_ + d] << 16);
      bp[r] = (unsigned)b2[s0] | ((unsigned)b2[s1] << 16);
    }
  }
#pragma unroll 1
  for (int g = NA; g < NG; ++g) {
    BAR();
    const char* cb = smem + (g % K3_S) * K3_SB;
    bf16x8 a[8], b[8];
#pragma unroll
    for (int ks = 0; ks < 8; ++ks) {
      const char* c2 = cb + (ks >> 2) * 16384;
      a[ks] = read_ks64(c2, wm, ks & 3, lane);
      b[ks] = read_kc(c2 + 8192, wn * 32 + (lane & 31), ks & 3, h);
    }
    __builtin_amdgcn_sched_barrier(0);
#pragma unroll
    for (int ks = 0; ks < 8; ++ks) acc2 = MFMA32(a[ks], b[ks], acc2);
  }
  STAMP(2);
  int lz = lane;
  asm volatile("" : "+v"(lz));
  const int hz = lz >> 5, dz = dt * 64 + wn * 32 + (lz & 31);
  const float dsk = bf2f(Dsk[dz]);
#pragma unroll
  for (int r = 0; r < 16; ++r) {
    const int s = st * 64 + wm * 32 + crow(r, hz);
    const float xvr = __uint_as_float((r & 1) ? (xp[r >> 1] & 0xffff0000u) : (xp[r >> 1] << 16));
    const float xnr = __uint_as_float((r & 1) ? (xnp[r >> 1] & 0xffff0000u) : (xnp[r >> 1] << 16));
    const float bvr = __uint_as_float((r & 1) ? (bp[r >> 1] & 0xffff0000u) : (bp[r >> 1] << 16));
    const float yA = 2.0f * acc1[r] + rbf(xnr * dsk);
    float yB = rbf(acc2[r]);
    yB = rbf(yB + bvr);
    const float gg = rbf(yB / (1.0f + __expf(-yB)));
    out[(long)s * D_ + dz] = xvr + yA * gg;
  }
  STAMP(3);
}

extern "C" void kernel_launch(void* const* d_in, const int* in_sizes, int n_in, void* d_out, int out_size, void* d_ws,
                              size_t ws_size, hipStream_t stream) {
  const bf16_t* x = (const bf16_t*)d_in[0];
  const bf16_t* Lre = (const bf16_t*)d_in[1];
  const bf16_t* Lim = (const bf16_t*)d_in[2];
  const bf16_t* Bre = (const bf16_t*)d_in[3];
  const bf16_t* Bim = (const bf16_t*)d_in[4];
  const bf16_t* Cre = (const bf16_t*)d_in[5];
  const bf16_t* Cim = (const bf16_t*)d_in[6];
  const bf16_t* logdt = (const bf16_t*)d_in[7];
  const bf16_t* Dsk = (const bf16_t*)d_in[8];
  const bf16_t* w1 = (const bf16_t*)d_in[10];
  const bf16_t* b1 = (const bf16_t*)d_in[11];
  const bf16_t* w2 = (const bf16_t*)d_in[12];
  const bf16_t* b2 = (const bf16_t*)d_in[13];
  float* out = (float*)d_out;

  char* ws = (char*)d_ws;
  bf16_t* xn = (bf16_t*)(ws);
  bf16_t* hn = (bf16_t*)(ws + (2u << 20));
  bf16_t* hbuf = (bf16_t*)(ws + (4u << 20));
  bf16_t* u = (bf16_t*)(ws + (12u << 20));
  float4* cst = (float4*)(ws + (14u << 20));
  u64* agg = (u64*)(ws + (14u << 20) + 65536);

  k1_ln<<<512, 256, 0, stream>>>(x, xn, hn, Lre, Lim, logdt, cst, (uint4*)agg);
  k2_gemm_scan<<<256, 768, 0, stream>>>(xn, hn, Bre, Bim, cst, agg, hbuf, w1, b1, u);
  k3_out<<<256, 768, 0, stream>>>(hbuf, Cre, Cim, u, w2, b2, x, xn, Dsk, out);
}
```

```cpp
#include <hip/hip_runtime.h>
#include <stdint.h>

typedef unsigned short bf16_t;
typedef __attribute__((ext_vector_type(8))) short bf16x8;
typedef __attribute__((ext_vector_type(4))) short s16x4;
typedef __attribute__((ext_vector_type(16))) float f32x16;
typedef unsigned long long u64;

#define DI __device__ __forceinline__
#define GAS __attribute__((address_space(1)))
#define LAS __attribute__((address_space(3)))

constexpr int L_ = 2048;
constexpr int D_ = 512;
constexpr int P_ = 512;
constexpr int F_ = 1792;

DI float bf2f(bf16_t u) { return __uint_as_float(((unsigned)u) << 16); }
DI bf16_t f2bf(float f) { __bf16 b = (__bf16)f; return __builtin_bit_cast(bf16_t, b); }
DI float rbf(float f) { return bf2f(f2bf(f)); }

DI void glds16(const void* g, void* l) {
  __builtin_amdgcn_global_load_lds((const unsigned*)g, (unsigned*)l, 16, 0, 0);
}
#define MFMA32(a, b, c) __builtin_amdgcn_mfma_f32_32x32x16_bf16((a), (b), (c), 0, 0, 0)
#define BAR() __builtin_amdgcn_s_barrier()

template <int N> DI void wait_vm_c() { asm volatile("s_waitcnt vmcnt(%0)" ::"n"(N) : "memory"); }
template <int N> DI void wait_vm_chain(int n) {
  if constexpr (N <= 0) wait_vm_c<0>();
  else { if (n >= N) wait_vm_c<N>(); else wait_vm_chain<N - 2>(n); }
}
template <int MAXN> DI void wait_vm(int n) { wait_vm_chain<MAXN>(n); }

DI int crow(int reg, int h) { return (reg & 3) + 8 * (reg >> 2) + 4 * h; }

template <int R>
DI void stage_kc(const bf16_t* __restrict__ g, long ld, char* lds, int wave, int lane) {
  constexpr int PPW = R / 32;
#pragma unroll
  for (int i = 0; i < PPW; ++i) {
    const int piece = wave * PPW + i;
    const int row = piece * 8 + (lane >> 3);
    const int c = (lane & 7) ^ ((row >> 1) & 7);
    glds16(g + (long)row * ld + c * 8, lds + piece * 1024 + lane * 16);
  }
}
DI bf16x8 read_kc(const char* lds, int row, int ks, int h) {
  return *reinterpret_cast<const bf16x8*>(lds + row * 128 + (((2 * ks + h) ^ ((row >> 1) & 7)) << 4));
}
DI void stage_ks64(const bf16_t* __restrict__ g, long ld, char* lds, int wave, int lane) {
#pragma unroll
  for (int i = 0; i < 2; ++i) {
    const int piece = wave * 2 + i;
    const int k = piece * 8 + ((lane >> 2) & 7);
    const int col = (lane >> 5) * 32 + (lane & 3) * 8;
    glds16(g + (long)k * ld + col, lds + piece * 1024 + lane * 16);
  }
}
DI bf16x8 read_ks64(const char* lds, int cb, int ks, int lane) {
  const int h = lane >> 5, blk = (lane >> 4) & 1, q = (lane & 15) >> 2, p = lane & 3;
  const char* a = lds + (2 * ks + h) * 1024 + cb * 512 + q * 64 + blk * 32 + p * 8;
  s16x4 lo = __builtin_amdgcn_ds_read_tr16_b64_v4i16((LAS s16x4*)(a));
  s16x4 hi = __builtin_amdgcn_ds_read_tr16_b64_v4i16((LAS s16x4*)(a + 256));
  return __builtin_shufflevector(lo, hi, 0, 1, 2, 3, 4, 5, 6, 7);
}

template <int CTRL, int RMASK> DI float dpp_add(float v) {
  const int t = __builtin_amdgcn_update_dpp(0, __float_as_int(v), CTRL, RMASK, 0xf, true);
  return v + __int_as_float(t);
}
DI float wave_sum(float v) {
  v = dpp_add<0x111, 0xf>(v);
  v = dpp_add<0x112, 0xf>(v);
  v = dpp_add<0x114, 0xf>(v);
  v = dpp_add<0x118, 0xf>(v);
  v = dpp_add<0x142, 0xa>(v);
  v = dpp_add<0x143, 0xc>(v);
  return __int_as_float(__builtin_amdgcn_readlane(__float_as_int(v), 63));
}
DI void ln_row(const float (&v)[8], float (&o)[8]) {
  float s = 0.f;
#pragma unroll
  for (int i = 0; i < 8; ++i) s += v[i];
  const float mean = wave_sum(s) * (1.0f / 512.0f);
  float q = 0.f;
#pragma unroll
  for (int i = 0; i < 8; ++i) { const float d = v[i] - mean; q += d * d; }
  const float var = wave_sum(q) * (1.0f / 512.0f);
  const float mb = rbf(mean), vb = rbf(var);
  const float den = rbf(sqrtf(rbf(vb + 1e-5f)));
#pragma unroll
  for (int i = 0; i < 8; ++i) o[i] = rbf(rbf(v[i] - mb) / den);
}

__global__ __launch_bounds__(256) void k1_ln(const bf16_t* __restrict__ x, bf16_t* __restrict__ xn, bf16_t* __restrict__ hn,
                                             const bf16_t* __restrict__ Lre, const bf16_t* __restrict__ Lim,
                                             const bf16_t* __restrict__ logdt, float4* __restrict__ cst,
                                             uint4* __restrict__ aggz, const uint4* __restrict__ pf0,
                                             const uint4* __restrict__ pf1, unsigned* __restrict__ sink) {
  const int tid = threadIdx.x, lane = tid & 63, w = tid >> 6, bid = blockIdx.x;
  if (bid >= 512) {
    constexpr int N16 = L_ * F_ / 8;
    unsigned acc = 0;
    for (int i = (bid - 512) * 256 + tid; i < N16; i += 256 * 256) { const uint4 a = pf0[i], b = pf1[i]; acc += a.x ^ b.y; }
    if (acc == 0x9e3779b9u) sink[0] = acc;
    return;
  }
  if (tid < 32) aggz[bid * 32 + tid] = make_uint4(0u, 0u, 0u, 0u);
  if (bid < 2) {
    const int p = bid * 256 + tid;
    const float lre = bf2f(Lre[p]), lim = bf2f(Lim[p]);
    const float dt = rbf(expf(bf2f(logdt[p])));
    const float zr = dt * lre, zi = dt * lim;
    const float e = expf(zr);
    float sn, cs;
    sincosf(zi, &sn, &cs);
    const float br = e * cs, bi = e * sn;
    const float nr = br - 1.0f, ni = bi;
    const float den = lre * lre + lim * lim;
    cst[p] = make_float4(br, bi, (nr * lre + ni * lim) / den, (ni * lre - nr * lim) / den);
  }
  const int row = bid * 4 + w;
  const uint4 raw = reinterpret_cast<const uint4*>(x + (long)row * D_)[lane];
  const unsigned rw[4] = {raw.x, raw.y, raw.z, raw.w};
  float v[8], a[8], b[8];
#pragma unroll
  for (int i = 0; i < 4; ++i) { v[2 * i] = __uint_as_float(rw[i] << 16); v[2 * i + 1] = __uint_as_float(rw[i] & 0xffff0000u); }
  ln_row(v, a);
  ln_row(a, b);
  unsigned pa[4], pb[4];
#pragma unroll
  for (int i = 0; i < 4; ++i) {
    pa[i] = (unsigned)f2bf(a[2 * i]) | ((unsigned)f2bf(a[2 * i + 1]) << 16);
    pb[i] = (unsigned)f2bf(b[2 * i]) | ((unsigned)f2bf(b[2 * i + 1]) << 16);
  }
  reinterpret_cast<uint4*>(xn + (long)row * D_)[lane] = make_uint4(pa[0], pa[1], pa[2], pa[3]);
  reinterpret_cast<uint4*>(hn + (long)row * D_)[lane] = make_uint4(pb[0], pb[1], pb[2], pb[3]);
}

constexpr int NB1 = 224;
#define STAMP(i)
#define STAMP2(i)
constexpr int NLW = 8;
template <int R>
DI void stage_kc8(const bf16_t* __restrict__ g, long ld, char* lds, int wave, int lane) {
  constexpr int PPW = R / 8 / NLW;
#pragma unroll
  for (int i = 0; i < PPW; ++i) {
    const int piece = wave * PPW + i;
    const int row = piece * 8 + (lane >> 3);
    const int c = (lane & 7) ^ ((row >> 1) & 7);
    glds16(g + (long)row * ld + c * 8, lds + piece * 1024 + lane * 16);
  }
}
DI void stage_ks8(const bf16_t* __restrict__ g, long ld, char* lds, int wave, int lane) {
  const int k = wave * 8 + ((lane >> 2) & 7);
  const int col = (lane >> 5) * 32 + (lane & 3) * 8;
  glds16(g + (long)k * ld + col, lds + wave * 1024 + lane * 16);
}

struct cplx { float r, i; };
DI cplx cmul(cplx a, cplx b) { return {a.r * b.r - a.i * b.i, a.r * b.i + a.i * b.r}; }
DI cplx cfma(cplx a, cplx b, cplx c) { return {c.r + a.r * b.r - a.i * b.i, c.i + a.r * b.i + a.i * b.r}; }

constexpr int K2_RING = 98304;
constexpr int K2_X = K2_RING;
constexpr int K2_AG = K2_X + 32768;
constexpr int K2_E = K2_AG + 16384;
constexpr int K2_CNT = K2_E + 4096;
constexpr int K2_LDS = K2_CNT + 16;

__global__ __launch_bounds__(768) void k2_gemm_scan(const bf16_t* __restrict__ xn, const bf16_t* __restrict__ hn,
                                                   const bf16_t* __restrict__ Bre, const bf16_t* __restrict__ Bim,
                                                   const float4* __restrict__ cst, u64* agg, bf16_t* __restrict__ hbuf,
                                                   const bf16_t* __restrict__ w1, const bf16_t* __restrict__ b1,
                                                   bf16_t* __restrict__ u) {
  __shared__ __attribute__((aligned(16))) char smem[K2_LDS];
  const int tid = threadIdx.x, lane = tid & 63, wave = __builtin_amdgcn_readfirstlane(tid >> 6);
  const int bid = blockIdx.x;
  const int tb = bid >> 3, cg = bid & 7;
  const bool hasB = bid < NB1;
  const int lin = (bid & 7) * 28 + (bid >> 3);
  const int ft = lin >> 3, dt = lin & 7;
  constexpr int NTA = D_ / 64, SA = 24576;
  constexpr int NTB = L_ / 128, SBB = 32768;
  STAMP2(0);
  if (tid == 0) *reinterpret_cast<unsigned*>(smem + K2_CNT) = 0u;
  if (wave >= 4) {
    const int lw = wave - 4;
    const bf16_t* Ag = xn + (long)tb * 64 * D_;
    auto issueA = [&](int t) {
      char* nb = smem + (t & 3) * SA;
      stage_kc8<64>(Ag + t * 64, D_, nb, lw, lane);
      stage_kc8<64>(Bre + (long)cg * 64 * D_ + t * 64, D_, nb + 8192, lw, lane);
      stage_kc8<64>(Bim + (long)cg * 64 * D_ + t * 64, D_, nb + 16384, lw, lane);
    };
    issueA(0); issueA(1); issueA(2);
    for (int t = 0; t < NTA; ++t) {
      wait_vm<6>(3 * min(2, NTA - 1 - t));
      BAR();
      if (t + 3 < NTA) issueA(t + 3);
    }
    BAR();
    if (hasB) {
      const bf16_t* Hg = hn + dt * 64;
      const bf16_t* Wg = w1 + ft * 64;
      auto issueB = [&](int t) {
        char* nb = smem + (t % 3) * SBB;
#pragma unroll
        for (int hf = 0; hf < 2; ++hf) {
          stage_ks8(Hg + (long)(t * 128 + hf * 64) * D_, D_, nb + hf * 16384, lw, lane);
          stage_ks8(Wg + (long)(t * 128 + hf * 64) * F_, F_, nb + hf * 16384 + 8192, lw, lane);
        }
      };
      issueB(0); issueB(1);
      BAR();
      for (int t = 0; t < NTB; ++t) {
        wait_vm<4>(t + 1 < NTB ? 4 : 0);
        BAR();
        if (t + 2 < NTB) issueB(t + 2);
      }
    } else {
      BAR();
    }
    const int ch = lane;
    const float4 c4 = cst[cg * 64 + ch];
    const cplx lam = {c4.x, c4.y}, coef = {c4.z, c4.w};
    float2* AG = reinterpret_cast<float2*>(smem + K2_AG);
    const float2* E = reinterpret_cast<const float2*>(smem + K2_E);
    for (unsigned spins = 0;; ++spins) {
      u64 vr[4], vi[4];
#pragma unroll
      for (int k = 0; k < 4; ++k) {
        const int b = lw + 8 * k;
        GAS u64* ag = (GAS u64*)(agg + ((long)(b < tb ? b : 0) * P_ + cg * 64 + ch) * 2);
        vr[k] = __hip_atomic_load(ag, __ATOMIC_RELAXED, __HIP_MEMORY_SCOPE_AGENT);
        vi[k] = __hip_atomic_load(ag + 1, __ATOMIC_RELAXED, __HIP_MEMORY_SCOPE_AGENT);
      }
      bool ok = true;
#pragma unroll
      for (int k = 0; k < 4; ++k) {
        const int b = lw + 8 * k;
        if (b < tb) {
          ok = ok && ((vr[k] >> 32) == 1ull) && ((vi[k] >> 32) == 1ull);
          AG[b * 64 + ch] = make_float2(__uint_as_float((unsigned)vr[k]), __uint_as_float((unsigned)vi[k]));
        }
      }
      if (__all(ok)) break;
      if (spins > 200000u) break;
      __builtin_amdgcn_s_sleep(1);
    }
    __builtin_amdgcn_s_waitcnt(0xc07f);
    unsigned* cnt = reinterpret_cast<unsigned*>(smem + K2_CNT);
    if (lane == 0) atomicAdd(cnt, 1u);
    for (unsigned spins = 0; spins < 4000000u; ++spins) {
      if (*(volatile unsigned*)cnt >= 8u) break;
    }
    cplx L8 = lam;
#pragma unroll
    for (int i = 0; i < 3; ++i) L8 = cmul(L8, L8);
    const cplx L16 = cmul(L8, L8);
    cplx L64 = cmul(L16, L16);
    L64 = cmul(L64, L64);
    const int s16 = lw >> 1;
    cplx cin = {0.f, 0.f};
#pragma unroll
    for (int s = 0; s < 4; ++s) {
      if (s < s16) { const float2 e = E[s * 64 + ch]; cin = cfma(L16, cin, cplx{e.x, e.y}); }
    }
    cplx cb = {0.f, 0.f};
    for (int b = 0; b < tb; ++b) {
      const float2 e = AG[b * 64 + ch];
      cb = cfma(L64, cb, cplx{e.x, e.y});
    }
    cplx Pw = {1.f, 0.f};
    for (int s = 0; s < s16; ++s) Pw = cmul(Pw, L16);
    const cplx cs = cfma(Pw, cb, cin);
    cplx p = (lw & 1) ? cmul(L8, lam) : lam;
    const float* Xr = reinterpret_cast<const float*>(smem + K2_X) + lw * 8 * 64 + ch;
    const float* Xi = Xr + 64 * 64;
    bf16_t* hrow = hbuf + ((long)tb * 64 + lw * 8) * 2048 + cg * 64 + ch;
#pragma unroll
    for (int j = 0; j < 8; ++j) {
      cplx hv = {Xr[j * 64], Xi[j * 64]};
      hv = cfma(p, cs, hv);
      p = cmul(p, lam);
      hv = cmul(coef, hv);
      const float re = hv.r, nim = -hv.i;
      const bf16_t rh = f2bf(re), ih = f2bf(nim);
      bf16_t* o = hrow + (long)j * 2048;
      o[0] = rh;
      o[512] = ih;
      o[1024] = f2bf(re - bf2f(rh));
      o[1536] = f2bf(nim - bf2f(ih));
    }
    return;
  }
  const int h = lane >> 5;
  const int wm = wave >> 1, wn = wave & 1;
  {
    f32x16 acc[2];
#pragma unroll
    for (int i = 0; i < 16; ++i) { acc[0][i] = 0.f; acc[1][i] = 0.f; }
#pragma unroll 1
    for (int t = 0; t < NTA; ++t) {
      BAR();
      const char* cb = smem + (t & 3) * SA;
      bf16x8 a[4], b[4][2];
#pragma unroll
      for (int ks = 0; ks < 4; ++ks) {
        a[ks] = read_kc(cb, wm * 32 + (lane & 31), ks, h);
#pragma unroll
        for (int n = 0; n < 2; ++n) b[ks][n] = read_kc(cb + 8192 + wn * 8192, n * 32 + (lane & 31), ks, h);
      }
      __builtin_amdgcn_sched_barrier(0);
#pragma unroll
      for (int ks = 0; ks < 4; ++ks)
#pragma unroll
        for (int n = 0; n < 2; ++n) acc[n] = MFMA32(a[ks], b[ks][n], acc[n]);
    }
    float* X = reinterpret_cast<float*>(smem + K2_X);
#pragma unroll
    for (int n = 0; n < 2; ++n)
#pragma unroll
      for (int r = 0; r < 16; ++r) {
        const int row = wm * 32 + crow(r, h);
        X[(wn * 64 + row) * 64 + n * 32 + (lane & 31)] = acc[n][r];
      }
  }
  __builtin_amdgcn_s_waitcnt(0xc07f);
  BAR();
  const int ch = lane, seg = wave;
  const float4 c4 = cst[cg * 64 + ch];
  const cplx lam = {c4.x, c4.y}, coef = {c4.z, c4.w};
  float* Xr = reinterpret_cast<float*>(smem + K2_X) + seg * 16 * 64 + ch;
  float* Xi = Xr + 64 * 64;
  float2* E = reinterpret_cast<float2*>(smem + K2_E);
  {
    cplx g = {0.f, 0.f};
#pragma unroll
    for (int j = 0; j < 16; ++j) {
      const cplx xv = {Xr[j * 64], Xi[j * 64]};
      g = cfma(lam, g, xv);
      Xr[j * 64] = g.r;
      Xi[j * 64] = g.i;
    }
    E[seg * 64 + ch] = make_float2(g.r, g.i);
  }
  __builtin_amdgcn_s_waitcnt(0xc07f);
  BAR();
  cplx L16 = lam;
#pragma unroll
  for (int i = 0; i < 4; ++i) L16 = cmul(L16, L16);
  cplx cin = {0.f, 0.f};
  {
    cplx c = {0.f, 0.f};
#pragma unroll
    for (int s = 0; s < 4; ++s) {
      if (s == seg) cin = c;
      const float2 e = E[s * 64 + ch];
      c = cfma(L16, c, cplx{e.x, e.y});
    }
    if (seg == 0) {
      GAS u64* ag = (GAS u64*)(agg + ((long)tb * P_ + cg * 64 + ch) * 2);
      __hip_atomic_store(ag, (1ull << 32) | (u64)__float_as_uint(c.r), __ATOMIC_RELAXED, __HIP_MEMORY_SCOPE_AGENT);
      __hip_atomic_store(ag + 1, (1ull << 32) | (u64)__float_as_uint(c.i), __ATOMIC_RELAXED, __HIP_MEMORY_SCOPE_AGENT);
    }
  }
  STAMP2(1);
  f32x16 accB;
#pragma unroll
  for (int i = 0; i < 16; ++i) accB[i] = 0.f;
  if (hasB) {
#pragma unroll 1
    for (int t = 0; t < NTB; ++t) {
      BAR();
      const char* cb = smem + (t % 3) * SBB;
      bf16x8 a[8], b[8];
#pragma unroll
      for (int ks = 0; ks < 8; ++ks) {
        a[ks] = read_ks64(cb + (ks >> 2) * 16384, wm, ks & 3, lane);
        b[ks] = read_ks64(cb + (ks >> 2) * 16384 + 8192, wn, ks & 3, lane);
      }
      __builtin_amdgcn_sched_barrier(0);
#pragma unroll
      for (int ks = 0; ks < 8; ++ks) accB = MFMA32(a[ks], b[ks], accB);
    }
  }
  STAMP2(2);
  STAMP2(3);
  if (hasB) {
    const int f = ft * 64 + wn * 32 + (lane & 31);
    const float bias = bf2f(b1[f]);
#pragma unroll
    for (int r = 0; r < 16; ++r) {
      const int d = dt * 64 + wm * 32 + crow(r, h);
      float v = rbf(accB[r]);
      v = rbf(v + bias);
      const float inner = 0.7978845608028654f * (v + 0.044715f * v * v * v);
      const float th = 1.0f - 2.0f / (__expf(2.0f * inner) + 1.0f);
      u[(long)d * F_ + f] = f2bf(0.5f * v * (1.0f + th));
    }
  }
  STAMP2(4);
}

constexpr int K3_SB = 49152, K3_S = 3;
__global__ __launch_bounds__(768) void k3_out(const bf16_t* __restrict__ hbuf, const bf16_t* __restrict__ Cre,
                                              const bf16_t* __restrict__ Cim, const bf16_t* __restrict__ u,
                                              const bf16_t* __restrict__ w2, const bf16_t* __restrict__ b2,
                                              const bf16_t* __restrict__ x, const bf16_t* __restrict__ xn,
                                              const bf16_t* __restrict__ Dsk, float* __restrict__ out) {
  __shared__ __attribute__((aligned(16))) char smem[K3_SB * K3_S];
  const int tid = threadIdx.x, lane = tid & 63, wave = __builtin_amdgcn_readfirstlane(tid >> 6);
  const int bid = blockIdx.x;
  const int st = (bid & 7) * 4 + (bid >> 6), dt = (bid >> 3) & 7;
  constexpr int NA = 8, NB = 14, NG = NA + NB;
  STAMP(0);
  if (wave >= 4) {
    const int lw = wave - 4;
    const bf16_t* hA = hbuf + (long)st * 64 * 2048;
    auto issue = [&](int g) {
      char* nb = smem + (g % K3_S) * K3_SB;
      if (g < NA) {
#pragma unroll
        for (int hf = 0; hf < 2; ++hf) {
          const int kt = 2 * g + hf;
          stage_kc8<64>(hA + kt * 64, 2048, nb + hf * 24576, lw, lane);
          stage_kc8<64>(hA + 1024 + kt * 64, 2048, nb + hf * 24576 + 8192, lw, lane);
          stage_kc8<64>((kt < 8 ? Cre : Cim) + (long)dt * 64 * P_ + (kt & 7) * 64, P_, nb + hf * 24576 + 16384, lw, lane);
        }
      } else {
#pragma unroll
        for (int hf = 0; hf < 2; ++hf) {
          const int kt = 2 * (g - NA) + hf;
          stage_ks8(w2 + (long)kt * 64 * L_ + st * 64, L_, nb + hf * 16384, lw, lane);
          stage_kc8<64>(u + (long)dt * 64 * F_ + kt * 64, F_, nb + hf * 16384 + 8192, lw, lane);
        }
      }
    };
    auto gcount = [&](int g) { return g >= NG ? 0 : (g < NA ? 6 : 4); };
    issue(0);
    issue(1);
    for (int g = 0; g < NG; ++g) {
      wait_vm<6>(gcount(g + 1));
      BAR();
      if (g + K3_S - 1 < NG) issue(g + K3_S - 1);
    }
    return;
  }
  const int wm = wave >> 1, wn = wave & 1, h = lane >> 5;
  f32x16 acc1, acc2;
#pragma unroll
  for (int i = 0; i < 16; ++i) { acc1[i] = 0.f; acc2[i] = 0.f; }
#pragma unroll 1
  for (int g = 0; g < NA; ++g) {
    BAR();
    const char* cb = smem + (g % K3_S) * K3_SB;
#pragma unroll
    for (int hf = 0; hf < 2; ++hf) {
      const char* c2 = cb + hf * 24576;
      bf16x8 a0[4], a1[4], b[4];
#pragma unroll
      for (int ks = 0; ks < 4; ++ks) {
        a0[ks] = read_kc(c2, wm * 32 + (lane & 31), ks, h);
        a1[ks] = read_kc(c2 + 8192, wm * 32 + (lane & 31), ks, h);
        b[ks] = read_kc(c2 + 16384, wn * 32 + (lane & 31), ks, h);
      }
      __builtin_amdgcn_sched_barrier(0);
#pragma unroll
      for (int ks = 0; ks < 4; ++ks) {
        acc1 = MFMA32(a0[ks], b[ks], acc1);
        acc1 = MFMA32(a1[ks], b[ks], acc1);
      }
      __builtin_amdgcn_sched_barrier(0);
    }
  }
  STAMP(1);
  unsigned xp[8], xnp[8], bp[8];
  {
    const int d = dt * 64 + wn * 32 + (lane & 31);
#pragma unroll
    for (int r = 0; r < 8; ++r) {
      const int s0 = st * 64 + wm * 32 + crow(2 * r, h), s1 = st * 64 + wm * 32 + crow(2 * r + 1, h);
      xp[r] = (unsigned)x[(long)s0 * D_ + d] | ((unsigned)x[(long)s1 * D_ + d] << 16);
      xnp[r] = (unsigned)xn[(long)s0 * D_ + d] | ((unsigned)xn[(long)s1 * D_ + d] << 16);
      bp[r] = (unsigned)b2[s0] | ((unsigned)b2[s1] << 16);
    }
  }
#pragma unroll 1
  for (int g = NA; g < NG; ++g) {
    BAR();
    const char* cb = smem + (g % K3_S) * K3_SB;
    bf16x8 a[8], b[8];
#pragma unroll
    for (int ks = 0; ks < 8; ++ks) {
      const char* c2 = cb + (ks >> 2) * 16384;
      a[ks] = read_ks64(c2, wm, ks & 3, lane);
      b[ks] = read_kc(c2 + 8192, wn * 32 + (lane & 31), ks & 3, h);
    }
    __builtin_amdgcn_sched_barrier(0);
#pragma unroll
    for (int ks = 0; ks < 8; ++ks) acc2 = MFMA32(a[ks], b[ks], acc2);
  }
  STAMP(2);
  int lz = lane;
  asm volatile("" : "+v"(lz));
  const int hz = lz >> 5, dz = dt * 64 + wn * 32 + (lz & 31);
  const float dsk = bf2f(Dsk[dz]);
#pragma unroll
  for (int r = 0; r < 16; ++r) {
    const int s = st * 64 + wm * 32 + crow(r, hz);
    const float xvr = __uint_as_float((r & 1) ? (xp[r >> 1] & 0xffff0000u) : (xp[r >> 1] << 16));
    const float xnr = __uint_as_float((r & 1) ? (xnp[r >> 1] & 0xffff0000u) : (xnp[r >> 1] << 16));
    const float bvr = __uint_as_float((r & 1) ? (bp[r >> 1] & 0xffff0000u) : (bp[r >> 1] << 16));
    const float yA = 2.0f * acc1[r] + rbf(xnr * dsk);
    float yB = rbf(acc2[r]);
    yB = rbf(yB + bvr);
    const float gg = rbf(yB / (1.0f + __expf(-yB)));
    out[(long)s * D_ + dz] = xvr + yA * gg;
  }
  STAMP(3);
}

extern "C" void kernel_launch(void* const* d_in, const int* in_sizes, int n_in, void* d_out, int out_size, void* d_ws,
                              size_t ws_size, hipStream_t stream) {
  const bf16_t* x = (const bf16_t*)d_in[0];
  const bf16_t* Lre = (const bf16_t*)d_in[1];
  const bf16_t* Lim = (const bf16_t*)d_in[2];
  const bf16_t* Bre = (const bf16_t*)d_in[3];
  const bf16_t* Bim = (const bf16_t*)d_in[4];
  const bf16_t* Cre = (const bf16_t*)d_in[5];
  const bf16_t* Cim = (const bf16_t*)d_in[6];
  const bf16_t* logdt = (const bf16_t*)d_in[7];
  const bf16_t* Dsk = (const bf16_t*)d_in[8];
  const bf16_t* w1 = (const bf16_t*)d_in[10];
  const bf16_t* b1 = (const bf16_t*)d_in[11];
  const bf16_t* w2 = (const bf16_t*)d_in[12];
  const bf16_t* b2 = (const bf16_t*)d_in[13];
  float* out = (float*)d_out;

  char* ws = (char*)d_ws;
  bf16_t* xn = (bf16_t*)(ws);
  bf16_t* hn = (bf16_t*)(ws + (2u << 20));
  bf16_t* hbuf = (bf16_t*)(ws + (4u << 20));
  bf16_t* u = (bf16_t*)(ws + (12u << 20));
  float4* cst = (float4*)(ws + (14u << 20));
  u64* agg = (u64*)(ws + (14u << 20) + 65536);

    k1_ln<<<768, 256, 0, stream>>>(x, xn, hn, Lre, Lim, logdt, cst, (uint4*)agg, (const uint4*)w1, (const uint4*)w2, (unsigned*)(ws + (15u << 20)));
  k2_gemm_scan<<<256, 768, 0, stream>>>(xn, hn, Bre, Bim, cst, agg, hbuf, w1, b1, u);
  k3_out<<<256, 768, 0, stream>>>(hbuf, Cre, Cim, u, w2, b2, x, xn, Dsk, out);
}
```

```cpp
#include <hip/hip_runtime.h>
#include <stdint.h>

typedef unsigned short bf16_t;
typedef __attribute__((ext_vector_type(8))) short bf16x8;
typedef __attribute__((ext_vector_type(4))) short s16x4;
typedef __attribute__((ext_vector_type(16))) float f32x16;
typedef unsigned long long u64;

#define DI __device__ __forceinline__
#define GAS __attribute__((address_space(1)))
#define LAS __attribute__((address_space(3)))

constexpr int L_ = 2048;
constexpr int D_ = 512;
constexpr int P_ = 512;
constexpr int F_ = 1792;

DI float bf2f(bf16_t u) { return __uint_as_float(((unsigned)u) << 16); }
DI bf16_t f2bf(float f) { __bf16 b = (__bf16)f; return __builtin_bit_cast(bf16_t, b); }
DI float rbf(float f) { return bf2f(f2bf(f)); }

DI void glds16(const void* g, void* l) {
  __builtin_amdgcn_global_load_lds((const unsigned*)g, (unsigned*)l, 16, 0, 0);
}
#define MFMA32(a, b, c) __builtin_amdgcn_mfma_f32_32x32x16_bf16((a), (b), (c), 0, 0, 0)
#define BAR() __builtin_amdgcn_s_barrier()

template <int N> DI void wait_vm_c() { asm volatile("s_waitcnt vmcnt(%0)" ::"n"(N) : "memory"); }
template <int N> DI void wait_vm_chain(int n) {
  if constexpr (N <= 0) wait_vm_c<0>();
  else { if (n >= N) wait_vm_c<N>(); else wait_vm_chain<N - 2>(n); }
}
template <int MAXN> DI void wait_vm(int n) { wait_vm_chain<MAXN>(n); }

DI int crow(int reg, int h) { return (reg & 3) + 8 * (reg >> 2) + 4 * h; }

template <int R>
DI void stage_kc(const bf16_t* __restrict__ g, long ld, char* lds, int wave, int lane) {
  constexpr int PPW = R / 32;
#pragma unroll
  for (int i = 0; i < PPW; ++i) {
    const int piece = wave * PPW + i;
    const int row = piece * 8 + (lane >> 3);
    const int c = (lane & 7) ^ ((row >> 1) & 7);
    glds16(g + (long)row * ld + c * 8, lds + piece * 1024 + lane * 16);
  }
}
DI bf16x8 read_kc(const char* lds, int row, int ks, int h) {
  return *reinterpret_cast<const bf16x8*>(lds + row * 128 + (((2 * ks + h) ^ ((row >> 1) & 7)) << 4));
}
DI void stage_ks64(const bf16_t* __restrict__ g, long ld, char* lds, int wave, int lane) {
#pragma unroll
  for (int i = 0; i < 2; ++i) {
    const int piece = wave * 2 + i;
    const int k = piece * 8 + ((lane >> 2) & 7);
    const int col = (lane >> 5) * 32 + (lane & 3) * 8;
    glds16(g + (long)k * ld + col, lds + piece * 1024 + lane * 16);
  }
}
DI bf16x8 read_ks64(const char* lds, int cb, int ks, int lane) {
  const int h = lane >> 5, blk = (lane >> 4) & 1, q = (lane & 15) >> 2, p = lane & 3;
  const char* a = lds + (2 * ks + h) * 1024 + cb * 512 + q * 64 + blk * 32 + p * 8;
  s16x4 lo = __builtin_amdgcn_ds_read_tr16_b64_v4i16((LAS s16x4*)(a));
  s16x4 hi = __builtin_amdgcn_ds_read_tr16_b64_v4i16((LAS s16x4*)(a + 256));
  return __builtin_shufflevector(lo, hi, 0, 1, 2, 3, 4, 5, 6, 7);
}

template <int CTRL, int RMASK> DI float dpp_add(float v) {
  const int t = __builtin_amdgcn_update_dpp(0, __float_as_int(v), CTRL, RMASK, 0xf, true);
  return v + __int_as_float(t);
}
DI float wave_sum(float v) {
  v = dpp_add<0x111, 0xf>(v);
  v = dpp_add<0x112, 0xf>(v);
  v = dpp_add<0x114, 0xf>(v);
  v = dpp_add<0x118, 0xf>(v);
  v = dpp_add<0x142, 0xa>(v);
  v = dpp_add<0x143, 0xc>(v);
  return __int_as_float(__builtin_amdgcn_readlane(__float_as_int(v), 63));
}
DI void ln_row(const float (&v)[8], float (&o)[8]) {
  float s = 0.f;
#pragma unroll
  for (int i = 0; i < 8; ++i) s += v[i];
  const float mean = wave_sum(s) * (1.0f / 512.0f);
  float q = 0.f;
#pragma unroll
  for (int i = 0; i < 8; ++i) { const float d = v[i] - mean; q += d * d; }
  const float var = wave_sum(q) * (1.0f / 512.0f);
  const float mb = rbf(mean), vb = rbf(var);
  const float den = rbf(sqrtf(rbf(vb + 1e-5f)));
#pragma unroll
  for (int i = 0; i < 8; ++i) o[i] = rbf(rbf(v[i] - mb) / den);
}

__global__ __launch_bounds__(256) void k1_ln(const bf16_t* __restrict__ x, bf16_t* __restrict__ xn, bf16_t* __restrict__ hn,
                                             const bf16_t* __restrict__ Lre, const bf16_t* __restrict__ Lim,
                                             const bf16_t* __restrict__ logdt, float4* __restrict__ cst,
                                             uint4* __restrict__ aggz, const uint4* __restrict__ pf0,
                                             const uint4* __restrict__ pf1, const uint4* __restrict__ pf2,
                                             const uint4* __restrict__ pf3, const uint4* __restrict__ pf4,
                                             const uint4* __restrict__ pf5, unsigned* __restrict__ sink) {
  const int tid = threadIdx.x, lane = tid & 63, w = tid >> 6, bid = blockIdx.x;
  if (bid >= 512) {
    constexpr int N16 = L_ * F_ / 8;
    unsigned acc = 0;
    for (int i = (bid - 512) * 256 + tid; i < N16; i += 256 * 256) { const uint4 a = pf0[i], b = pf1[i]; acc += a.x ^ b.y; }
    {
      const int i = (bid - 512) * 256 + tid;
      if (i < P_ * D_ / 8) { const uint4 a = pf2[i], b = pf3[i], c = pf4[i], d = pf5[i]; acc += a.x ^ b.y ^ c.z ^ d.w; }
    }
    if (acc == 0x9e3779b9u) sink[0] = acc;
    return;
  }
  if (tid < 32) aggz[bid * 32 + tid] = make_uint4(0u, 0u, 0u, 0u);
  if (bid < 2) {
    const int p = bid * 256 + tid;
    const float lre = bf2f(Lre[p]), lim = bf2f(Lim[p]);
    const float dt = rbf(expf(bf2f(logdt[p])));
    const float zr = dt * lre, zi = dt * lim;
    const float e = expf(zr);
    float sn, cs;
    sincosf(zi, &sn, &cs);
    const float br = e * cs, bi = e * sn;
    const float nr = br - 1.0f, ni = bi;
    const float den = lre * lre + lim * lim;
    cst[p] = make_float4(br, bi, (nr * lre + ni * lim) / den, (ni * lre - nr * lim) / den);
  }
  const int row = bid * 4 + w;
  const uint4 raw = reinterpret_cast<const uint4*>(x + (long)row * D_)[lane];
  const unsigned rw[4] = {raw.x, raw.y, raw.z, raw.w};
  float v[8], a[8], b[8];
#pragma unroll
  for (int i = 0; i < 4; ++i) { v[2 * i] = __uint_as_float(rw[i] << 16); v[2 * i + 1] = __uint_as_float(rw[i] & 0xffff0000u); }
  ln_row(v, a);
  ln_row(a, b);
  unsigned pa[4], pb[4];
#pragma unroll
  for (int i = 0; i < 4; ++i) {
    pa[i] = (unsigned)f2bf(a[2 * i]) | ((unsigned)f2bf(a[2 * i + 1]) << 16);
    pb[i] = (unsigned)f2bf(b[2 * i]) | ((unsigned)f2bf(b[2 * i + 1]) << 16);
  }
  reinterpret_cast<uint4*>(xn + (long)row * D_)[lane] = make_uint4(pa[0], pa[1], pa[2], pa[3]);
  reinterpret_cast<uint4*>(hn + (long)row * D_)[lane] = make_uint4(pb[0], pb[1], pb[2], pb[3]);
}

constexpr int NB1 = 224;
#define STAMP(i)
#define STAMP2(i)
constexpr int NLW = 8;
template <int R>
DI void stage_kc8(const bf16_t* __restrict__ g, long ld, char* lds, int wave, int lane) {
  constexpr int PPW = R / 8 / NLW;
#pragma unroll
  for (int i = 0; i < PPW; ++i) {
    const int piece = wave * PPW + i;
    const int row = piece * 8 + (lane >> 3);
    const int c = (lane & 7) ^ ((row >> 1) & 7);
    glds16(g + (long)row * ld + c * 8, lds + piece * 1024 + lane * 16);
  }
}
DI void stage_ks8(const bf16_t* __restrict__ g, long ld, char* lds, int wave, int lane) {
  const int k = wave * 8 + ((lane >> 2) & 7);
  const int col = (lane >> 5) * 32 + (lane & 3) * 8;
  glds16(g + (long)k * ld + col, lds + wave * 1024 + lane * 16);
}

struct cplx { float r, i; };
DI cplx cmul(cplx a, cplx b) { return {a.r * b.r - a.i * b.i, a.r * b.i + a.i * b.r}; }
DI cplx cfma(cplx a, cplx b, cplx c) { return {c.r + a.r * b.r - a.i * b.i, c.i + a.r * b.i + a.i * b.r}; }

constexpr int K2_RING = 98304;
constexpr int K2_X = K2_RING;
constexpr int K2_AG = K2_X + 32768;
constexpr int K2_E = K2_AG + 16384;
constexpr int K2_CNT = K2_E + 4096;
constexpr int K2_LDS = K2_CNT + 16;

__global__ __launch_bounds__(768) void k2_gemm_scan(const bf16_t* __restrict__ xn, const bf16_t* __restrict__ hn,
                                                   const bf16_t* __restrict__ Bre, const bf16_t* __restrict__ Bim,
                                                   const float4* __restrict__ cst, u64* agg, bf16_t* __restrict__ hbuf,
                                                   const bf16_t* __restrict__ w1, const bf16_t* __restrict__ b1,
                                                   bf16_t* __restrict__ u) {
  __shared__ __attribute__((aligned(16))) char smem[K2_LDS];
  const int tid = threadIdx.x, lane = tid & 63, wave = __builtin_amdgcn_readfirstlane(tid >> 6);
  const int bid = blockIdx.x;
  const int tb = bid >> 3, cg = bid & 7;
  const bool hasB = bid < NB1;
  const int lin = (bid & 7) * 28 + (bid >> 3);
  const int ft = lin >> 3, dt = lin & 7;
  constexpr int NTA = D_ / 64, SA = 24576;
  constexpr int NTB = L_ / 128, SBB = 32768;
  STAMP2(0);
  if (tid == 0) *reinterpret_cast<unsigned*>(smem + K2_CNT) = 0u;
  if (wave >= 4) {
    const int lw = wave - 4;
    const bf16_t* Ag = xn + (long)tb * 64 * D_;
    auto issueA = [&](int t) {
      char* nb = smem + (t & 3) * SA;
      stage_kc8<64>(Ag + t * 64, D_, nb, lw, lane);
      stage_kc8<64>(Bre + (long)cg * 64 * D_ + t * 64, D_, nb + 8192, lw, lane);
      stage_kc8<64>(Bim + (long)cg * 64 * D_ + t * 64, D_, nb + 16384, lw, lane);
    };
    issueA(0); issueA(1); issueA(2);
    for (int t = 0; t < NTA; ++t) {
      wait_vm<6>(3 * min(2, NTA - 1 - t));
      BAR();
      if (t + 3 < NTA) issueA(t + 3);
    }
    BAR();
    if (hasB) {
      const bf16_t* Hg = hn + dt * 64;
      const bf16_t* Wg = w1 + ft * 64;
      auto issueB = [&](int t) {
        char* nb = smem + (t % 3) * SBB;
#pragma unroll
        for (int hf = 0; hf < 2; ++hf) {
          stage_ks8(Hg + (long)(t * 128 + hf * 64) * D_, D_, nb + hf * 16384, lw, lane);
          stage_ks8(Wg + (long)(t * 128 + hf * 64) * F_, F_, nb + hf * 16384 + 8192, lw, lane);
        }
      };
      issueB(0); issueB(1);
      BAR();
      for (int t = 0; t < NTB; ++t) {
        wait_vm<4>(t + 1 < NTB ? 4 : 0);
        BAR();
        if (t + 2 < NTB) issueB(t + 2);
      }
    } else {
      BAR();
    }
    const int ch = lane;
    const float4 c4 = cst[cg * 64 + ch];
    const cplx lam = {c4.x, c4.y}, coef = {c4.z, c4.w};
    float2* AG = reinterpret_cast<float2*>(smem + K2_AG);
    const float2* E = reinterpret_cast<const float2*>(smem + K2_E);
    for (unsigned spins = 0;; ++spins) {
      u64 vr[4], vi[4];
#pragma unroll
      for (int k = 0; k < 4; ++k) {
        const int b = lw + 8 * k;
        GAS u64* ag = (GAS u64*)(agg + ((long)(b < tb ? b : 0) * P_ + cg * 64 + ch) * 2);
        vr[k] = __hip_atomic_load(ag, __ATOMIC_RELAXED, __HIP_MEMORY_SCOPE_AGENT);
        vi[k] = __hip_atomic_load(ag + 1, __ATOMIC_RELAXED, __HIP_MEMORY_SCOPE_AGENT);
      }
      bool ok = true;
#pragma unroll
      for (int k = 0; k < 4; ++k) {
        const int b = lw + 8 * k;
        if (b < tb) {
          ok = ok && ((vr[k] >> 32) == 1ull) && ((vi[k] >> 32) == 1ull);
          AG[b * 64 + ch] = make_float2(__uint_as_float((unsigned)vr[k]), __uint_as_float((unsigned)vi[k]));
        }
      }
      if (__all(ok)) break;
      if (spins > 200000u) break;
      __builtin_amdgcn_s_sleep(1);
    }
    __builtin_amdgcn_s_waitcnt(0xc07f);
    unsigned* cnt = reinterpret_cast<unsigned*>(smem + K2_CNT);
    if (lane == 0) atomicAdd(cnt, 1u);
    for (unsigned spins = 0; spins < 4000000u; ++spins) {
      if (*(volatile unsigned*)cnt >= 8u) break;
    }
    cplx L8 = lam;
#pragma unroll
    for (int i = 0; i < 3; ++i) L8 = cmul(L8, L8);
    const cplx L16 = cmul(L8, L8);
    cplx L64 = cmul(L16, L16);
    L64 = cmul(L64, L64);
    const int s16 = lw >> 1;
    cplx cin = {0.f, 0.f};
#pragma unroll
    for (int s = 0; s < 4; ++s) {
      if (s < s16) { const float2 e = E[s * 64 + ch]; cin = cfma(L16, cin, cplx{e.x, e.y}); }
    }
    cplx cb = {0.f, 0.f};
    for (int b = 0; b < tb; ++b) {
      const float2 e = AG[b * 64 + ch];
      cb = cfma(L64, cb, cplx{e.x, e.y});
    }
    cplx Pw = {1.f, 0.f};
    for (int s = 0; s < s16; ++s) Pw = cmul(Pw, L16);
    const cplx cs = cfma(Pw, cb, cin);
    cplx p = (lw & 1) ? cmul(L8, lam) : lam;
    const float* Xr = reinterpret_cast<const float*>(smem + K2_X) + lw * 8 * 64 + ch;
    const float* Xi = Xr + 64 * 64;
    bf16_t* hrow = hbuf + ((long)tb * 64 + lw * 8) * 2048 + cg * 64 + ch;
#pragma unroll
    for (int j = 0; j < 8; ++j) {
      cplx hv = {Xr[j * 64], Xi[j * 64]};
      hv = cfma(p, cs, hv);
      p = cmul(p, lam);
      hv = cmul(coef, hv);
      const float re = hv.r, nim = -hv.i;
      const bf16_t rh = f2bf(re), ih = f2bf(nim);
      bf16_t* o = hrow + (long)j * 2048;
      o[0] = rh;
      o[512] = ih;
      o[1024] = f2bf(re - bf2f(rh));
      o[1536] = f2bf(nim - bf2f(ih));
    }
    return;
  }
  const int h = lane >> 5;
  const int wm = wave >> 1, wn = wave & 1;
  {
    f32x16 acc[2];
#pragma unroll
    for (int i = 0; i < 16; ++i) { acc[0][i] = 0.f; acc[1][i] = 0.f; }
#pragma unroll 1
    for (int t = 0; t < NTA; ++t) {
      BAR();
      const char* cb = smem + (t & 3) * SA;
      bf16x8 a[4], b[4][2];
#pragma unroll
      for (int ks = 0; ks < 4; ++ks) {
        a[ks] = read_kc(cb, wm * 32 + (lane & 31), ks, h);
#pragma unroll
        for (int n = 0; n < 2; ++n) b[ks][n] = read_kc(cb + 8192 + wn * 8192, n * 32 + (lane & 31), ks, h);
      }
      __builtin_amdgcn_sched_barrier(0);
#pragma unroll
      for (int ks = 0; ks < 4; ++ks)
#pragma unroll
        for (int n = 0; n < 2; ++n) acc[n] = MFMA32(a[ks], b[ks][n], acc[n]);
    }
    float* X = reinterpret_cast<float*>(smem + K2_X);
#pragma unroll
    for (int n = 0; n < 2; ++n)
#pragma unroll
      for (int r = 0; r < 16; ++r) {
        const int row = wm * 32 + crow(r, h);
        X[(wn * 64 + row) * 64 + n * 32 + (lane & 31)] = acc[n][r];
      }
  }
  __builtin_amdgcn_s_waitcnt(0xc07f);
  BAR();
  const int ch = lane, seg = wave;
  const float4 c4 = cst[cg * 64 + ch];
  const cplx lam = {c4.x, c4.y}, coef = {c4.z, c4.w};
  float* Xr = reinterpret_cast<float*>(smem + K2_X) + seg * 16 * 64 + ch;
  float* Xi = Xr + 64 * 64;
  float2* E = reinterpret_cast<float2*>(smem + K2_E);
  {
    cplx g = {0.f, 0.f};
#pragma unroll
    for (int j = 0; j < 16; ++j) {
      const cplx xv = {Xr[j * 64], Xi[j * 64]};
      g = cfma(lam, g, xv);
      Xr[j * 64] = g.r;
      Xi[j * 64] = g.i;
    }
    E[seg * 64 + ch] = make_float2(g.r, g.i);
  }
  __builtin_amdgcn_s_waitcnt(0xc07f);
  BAR();
  cplx L16 = lam;
#pragma unroll
  for (int i = 0; i < 4; ++i) L16 = cmul(L16, L16);
  cplx cin = {0.f, 0.f};
  {
    cplx c = {0.f, 0.f};
#pragma unroll
    for (int s = 0; s < 4; ++s) {
      if (s == seg) cin = c;
      const float2 e = E[s * 64 + ch];
      c = cfma(L16, c, cplx{e.x, e.y});
    }
    if (seg == 0) {
      GAS u64* ag = (GAS u64*)(agg + ((long)tb * P_ + cg * 64 + ch) * 2);
      __hip_atomic_store(ag, (1ull << 32) | (u64)__float_as_uint(c.r), __ATOMIC_RELAXED, __HIP_MEMORY_SCOPE_AGENT);
      __hip_atomic_store(ag + 1, (1ull << 32) | (u64)__float_as_uint(c.i), __ATOMIC_RELAXED, __HIP_MEMORY_SCOPE_AGENT);
    }
  }
  STAMP2(1);
  f32x16 accB;
#pragma unroll
  for (int i = 0; i < 16; ++i) accB[i] = 0.f;
  const float bias = hasB ? bf2f(b1[ft * 64 + wn * 32 + (lane & 31)]) : 0.f;
  if (hasB) {
#pragma unroll 1
    for (int t = 0; t < NTB; ++t) {
      BAR();
      const char* cb = smem + (t % 3) * SBB;
      bf16x8 a[8], b[8];
#pragma unroll
      for (int ks = 0; ks < 8; ++ks) {
        a[ks] = read_ks64(cb + (ks >> 2) * 16384, wm, ks & 3, lane);
        b[ks] = read_ks64(cb + (ks >> 2) * 16384 + 8192, wn, ks & 3, lane);
      }
      __builtin_amdgcn_sched_barrier(0);
#pragma unroll
      for (int ks = 0; ks < 8; ++ks) accB = MFMA32(a[ks], b[ks], accB);
    }
  }
  STAMP2(2);
  STAMP2(3);
  if (hasB) {
    const int f = ft * 64 + wn * 32 + (lane & 31);
#pragma unroll
    for (int r = 0; r < 16; ++r) {
      const int d = dt * 64 + wm * 32 + crow(r, h);
      float v = rbf(accB[r]);
      v = rbf(v + bias);
      const float inner = 0.7978845608028654f * (v + 0.044715f * v * v * v);
      const float th = 1.0f - 2.0f / (__expf(2.0f * inner) + 1.0f);
      u[(long)d * F_ + f] = f2bf(0.5f * v * (1.0f + th));
    }
  }
  STAMP2(4);
}

constexpr int K3_SB = 49152, K3_S = 3;
__global__ __launch_bounds__(768) void k3_out(const bf16_t* __restrict__ hbuf, const bf16_t* __restrict__ Cre,
                                              const bf16_t* __restrict__ Cim, const bf16_t* __restrict__ u,
                                              const bf16_t* __restrict__ w2, const bf16_t* __restrict__ b2,
                                              const bf16_t* __restrict__ x, const bf16_t* __restrict__ xn,
                                              const bf16_t* __restrict__ Dsk, float* __restrict__ out) {
  __shared__ __attribute__((aligned(16))) char smem[K3_SB * K3_S];
  const int tid = threadIdx.x, lane = tid & 63, wave = __builtin_amdgcn_readfirstlane(tid >> 6);
  const int bid = blockIdx.x;
  const int st = (bid & 7) * 4 + (bid >> 6), dt = (bid >> 3) & 7;
  constexpr int NA = 8, NB = 14, NG = NA + NB;
  STAMP(0);
  if (wave >= 4) {
    const int lw = wave - 4;
    const bf16_t* hA = hbuf + (long)st * 64 * 2048;
    auto issue = [&](int g) {
      char* nb = smem + (g % K3_S) * K3_SB;
      if (g < NA) {
#pragma unroll
        for (int hf = 0; hf < 2; ++hf) {
          const int kt = 2 * g + hf;
          stage_kc8<64>(hA + kt * 64, 2048, nb + hf * 24576, lw, lane);
          stage_kc8<64>(hA + 1024 + kt * 64, 2048, nb + hf * 24576 + 8192, lw, lane);
          stage_kc8<64>((kt < 8 ? Cre : Cim) + (long)dt * 64 * P_ + (kt & 7) * 64, P_, nb + hf * 24576 + 16384, lw, lane);
        }
      } else {
#pragma unroll
        for (int hf = 0; hf < 2; ++hf) {
          const int kt = 2 * (g - NA) + hf;
          stage_ks8(w2 + (long)kt * 64 * L_ + st * 64, L_, nb + hf * 16384, lw, lane);
          stage_kc8<64>(u + (long)dt * 64 * F_ + kt * 64, F_, nb + hf * 16384 + 8192, lw, lane);
        }
      }
    };
    auto gcount = [&](int g) { return g >= NG ? 0 : (g < NA ? 6 : 4); };
    issue(0);
    issue(1);
    for (int g = 0; g < NG; ++g) {
      wait_vm<6>(gcount(g + 1));
      BAR();
      if (g + K3_S - 1 < NG) issue(g + K3_S - 1);
    }
    return;
  }
  const int wm = wave >> 1, wn = wave & 1, h = lane >> 5;
  f32x16 acc1, acc2;
#pragma unroll
  for (int i = 0; i < 16; ++i) { acc1[i] = 0.f; acc2[i] = 0.f; }
#pragma unroll 1
  for (int g = 0; g < NA; ++g) {
    BAR();
    const char* cb = smem + (g % K3_S) * K3_SB;
#pragma unroll
    for (int hf = 0; hf < 2; ++hf) {
      const char* c2 = cb + hf * 24576;
      bf16x8 a0[4], a1[4], b[4];
#pragma unroll
      for (int ks = 0; ks < 4; ++ks) {
        a0[ks] = read_kc(c2, wm * 32 + (lane & 31), ks, h);
        a1[ks] = read_kc(c2 + 8192, wm * 32 + (lane & 31), ks, h);
        b[ks] = read_kc(c2 + 16384, wn * 32 + (lane & 31), ks, h);
      }
      __builtin_amdgcn_sched_barrier(0);
#pragma unroll
      for (int ks = 0; ks < 4; ++ks) {
        acc1 = MFMA32(a0[ks], b[ks], acc1);
        acc1 = MFMA32(a1[ks], b[ks], acc1);
      }
      __builtin_amdgcn_sched_barrier(0);
    }
  }
  STAMP(1);
  unsigned xp[8], xnp[8], bp[8];
  float dsk;
  {
    const int d = dt * 64 + wn * 32 + (lane & 31);
    dsk = bf2f(Dsk[d]);
#pragma unroll
    for (int r = 0; r < 8; ++r) {
      const int s0 = st * 64 + wm * 32 + crow(2 * r, h), s1 = st * 64 + wm * 32 + crow(2 * r + 1, h);
      xp[r] = (unsigned)x[(long)s0 * D_ + d] | ((unsigned)x[(long)s1 * D_ + d] << 16);
      xnp[r] = (unsigned)xn[(long)s0 * D_ + d] | ((unsigned)xn[(long)s1 * D_ + d] << 16);
      bp[r] = (unsigned)b2[s0] | ((unsigned)b2[s1] << 16);
    }
  }
#pragma unroll 1
  for (int g = NA; g < NG; ++g) {
    BAR();
    const char* cb = smem + (g % K3_S) * K3_SB;
    bf16x8 a[8], b[8];
#pragma unroll
    for (int ks = 0; ks < 8; ++ks) {
      const char* c2 = cb + (ks >> 2) * 16384;
      a[ks] = read_ks64(c2, wm, ks & 3, lane);
      b[ks] = read_kc(c2 + 8192, wn * 32 + (lane & 31), ks & 3, h);
    }
    __builtin_amdgcn_sched_barrier(0);
#pragma unroll
    for (int ks = 0; ks < 8; ++ks) acc2 = MFMA32(a[ks], b[ks], acc2);
  }
  STAMP(2);
  int lz = lane;
  asm volatile("" : "+v"(lz));
  const int hz = lz >> 5, dz = dt * 64 + wn * 32 + (lz & 31);
#pragma unroll
  for (int r = 0; r < 16; ++r) {
    const int s = st * 64 + wm * 32 + crow(r, hz);
    const float xvr = __uint_as_float((r & 1) ? (xp[r >> 1] & 0xffff0000u) : (xp[r >> 1] << 16));
    const float xnr = __uint_as_float((r & 1) ? (xnp[r >> 1] & 0xffff0000u) : (xnp[r >> 1] << 16));
    const float bvr = __uint_as_float((r & 1) ? (bp[r >> 1] & 0xffff0000u) : (bp[r >> 1] << 16));
    const float yA = 2.0f * acc1[r] + rbf(xnr * dsk);
    float yB = rbf(acc2[r]);
    yB = rbf(yB + bvr);
    const float gg = rbf(yB / (1.0f + __expf(-yB)));
    out[(long)s * D_ + dz] = xvr + yA * gg;
  }
  STAMP(3);
}

extern "C" void kernel_launch(void* const* d_in, const int* in_sizes, int n_in, void* d_out, int out_size, void* d_ws,
                              size_t ws_size, hipStream_t stream) {
  const bf16_t* x = (const bf16_t*)d_in[0];
  const bf16_t* Lre = (const bf16_t*)d_in[1];
  const bf16_t* Lim = (const bf16_t*)d_in[2];
  const bf16_t* Bre = (const bf16_t*)d_in[3];
  const bf16_t* Bim = (const bf16_t*)d_in[4];
  const bf16_t* Cre = (const bf16_t*)d_in[5];
  const bf16_t* Cim = (const bf16_t*)d_in[6];
  const bf16_t* logdt = (const bf16_t*)d_in[7];
  const bf16_t* Dsk = (const bf16_t*)d_in[8];
  const bf16_t* w1 = (const bf16_t*)d_in[10];
  const bf16_t* b1 = (const bf16_t*)d_in[11];
  const bf16_t* w2 = (const bf16_t*)d_in[12];
  const bf16_t* b2 = (const bf16_t*)d_in[13];
  float* out = (float*)d_out;

  char* ws = (char*)d_ws;
  bf16_t* xn = (bf16_t*)(ws);
  bf16_t* hn = (bf16_t*)(ws + (2u << 20));
  bf16_t* hbuf = (bf16_t*)(ws + (4u << 20));
  bf16_t* u = (bf16_t*)(ws + (12u << 20));
  float4* cst = (float4*)(ws + (14u << 20));
  u64* agg = (u64*)(ws + (14u << 20) + 65536);

    k1_ln<<<768, 256, 0, stream>>>(x, xn, hn, Lre, Lim, logdt, cst, (uint4*)agg, (const uint4*)w1, (const uint4*)w2, (const uint4*)Bre, (const uint4*)Bim, (const uint4*)Cre, (const uint4*)Cim, (unsigned*)(ws + (15u << 20)));
  k2_gemm_scan<<<256, 768, 0, stream>>>(xn, hn, Bre, Bim, cst, agg, hbuf, w1, b1, u);
  k3_out<<<256, 768, 0, stream>>>(hbuf, Cre, Cim, u, w2, b2, x, xn, Dsk, out);
}
```
